```python
import torch

def _softmax(x, axis=-1):
    return torch.softmax(x, dim=axis)
def _gelu(x):
    return torch.nn.functional.gelu(x, approximate="tanh")
def _silu(x):
    return torch.nn.functional.silu(x)
def _sigmoid(x):
    return torch.sigmoid(x)
def _layernorm(x, axis=-1, eps=1e-5):
    m = x.mean(dim=axis, keepdim=True)
    v = x.var(dim=axis, keepdim=True, unbiased=False)
    return (x - m) / torch.sqrt(v + eps)
def _scatter_add(tgt, idx, src, axis=0):
    return tgt.clone().index_add_(axis, idx, src)
def _segment_sum(src, seg, n):
    out = torch.zeros((n,) + src.shape[1:], dtype=src.dtype)
    return out.index_add_(0, seg, src)
def _pad(x, pw):
    flat = tuple(v for pair in reversed(pw) for v in pair)
    return torch.nn.functional.pad(x, flat)

kernel_name = 'standalone__cheap_equivariant_egnn__graph'

def setup_inputs():

    _w = (lambda a: (lambda t: t.to(torch.bfloat16) if t.is_floating_point() else t)(torch.as_tensor(a)))
    N, deg, D, De = 10000, 64, 64, max(4, 1)
    E = N * deg
    return {
        "h": _w((lambda *s: torch.randn(*s, dtype=torch.bfloat16) * 0.2)(N, D)),
        "pos": _w((lambda *s: torch.randn(*s, dtype=torch.bfloat16) * 0.8)(N, 3)),
        "e_attr": _w((lambda *s: torch.randn(*s, dtype=torch.bfloat16) * 0.8)(E, De)),
        "src": _w((lambda lo, hi, *s: torch.randint(lo, hi, s))(0, N, E).to(torch.int64)),
        "dst": _w((lambda lo, hi, *s: torch.randint(lo, hi, s))(0, N, E).to(torch.int64)),
        "w_e": _w(0.1 * (lambda *s: torch.randn(*s, dtype=torch.bfloat16) * 0.8)(2 * D + 1 + De, D)),
        "w_x": _w(0.1 * (lambda *s: torch.randn(*s, dtype=torch.bfloat16) * 0.2)(D, 1)),
        "w_h": _w(0.1 * (lambda *s: torch.randn(*s, dtype=torch.bfloat16) * 0.2)(2 * D, D)),
    }

def reference(h, pos, e_attr, src, dst, w_e, w_x, w_h):
    N, D = h.shape[0], h.shape[1]
    diff = pos[dst] - pos[src]
    dist2 = torch.sum(diff * diff, axis=-1, keepdims=True)
    e_in = torch.concatenate([h[dst], h[src], dist2, e_attr], axis=-1)
    m_ij = _silu(e_in @ w_e)
    coord_scale = m_ij @ w_x
    pos_new = pos + _segment_sum(diff * coord_scale, dst, N)
    m_i = _segment_sum(m_ij, dst, N)
    h_new = h + _silu(torch.concatenate([h, m_i], axis=-1) @ w_h)
    return h_new, pos_new
```

```cpp
#include <hip/hip_runtime.h>
#include <hip/hip_bf16.h>
#include <stdint.h>

typedef unsigned int u32;
typedef unsigned short u16;
typedef __attribute__((ext_vector_type(4))) u32 u32x4;
typedef __attribute__((ext_vector_type(2))) u32 u32x2;
typedef __attribute__((ext_vector_type(8))) short s16x8;
typedef __attribute__((ext_vector_type(4))) short s16x4;
typedef __attribute__((ext_vector_type(16))) float f32x16;
typedef __attribute__((ext_vector_type(4))) float f32x4;
typedef __attribute__((ext_vector_type(2))) float f32x2;
typedef __attribute__((ext_vector_type(2))) __bf16 bf16x2;
#define LAS __attribute__((address_space(3)))
#define DI __device__ __forceinline__

struct __attribute__((packed, aligned(4))) rec12 { u32 a, b, c; };

constexpr int NP = 256;
constexpr int NBK = 256;
constexpr int K1T = 640;
constexpr int CHUNK_MAX = 2560;
constexpr int K2T = 512;
constexpr int NWIN = 5;
constexpr int CAPT = 90;
constexpr int CAP = CAPT * 32;
constexpr int NMAX = 10064;

constexpr int WF_W1 = 0;
constexpr int WF_W2 = 512;
constexpr int WF_WX = 1024;
constexpr int WF_WH = 1088;
constexpr int WF_TOTAL = 2112;
constexpr int WBLK = (WF_TOTAL + K1T - 1) / K1T;

constexpr int L_R1 = 0;
constexpr int L_R2 = L_R1 + CAP * 16;
constexpr int L_POS = L_R2 + CAP * 8;
constexpr int POS_BYTES = 60416;
constexpr int L_MACC = L_POS + POS_BYTES;
constexpr int L_PACC = L_MACC + 40 * 64 * 4;
constexpr int L_ANT = L_PACC + 40 * 96 * 4;
constexpr int L_OH = L_ANT + NWIN * 64 * 16;
constexpr int L_CNT = L_OH + 256;
constexpr int L_FLAG = L_CNT + NWIN * 32 * 4;
constexpr int L_TOTAL = L_FLAG + 16;
static_assert(L_TOTAL <= 163840, "LDS budget");

DI float bf_lo(u32 v) { return __uint_as_float(v << 16); }
DI float bf_hi(u32 v) { return __uint_as_float(v & 0xffff0000u); }
DI u32 pack_bf(float a, float b) {
  bf16x2 r; r.x = (__bf16)a; r.y = (__bf16)b;
  return __builtin_bit_cast(u32, r);
}
DI float round_bf(float a) { return (float)(__bf16)a; }
DI float silu_f(float x) {
  return x * __builtin_amdgcn_rcpf(1.0f + __builtin_amdgcn_exp2f(x * -1.4426950408889634f));
}
template <typename T> DI T lds_ld(u32 a) { return *(const LAS T*)(uintptr_t)a; }
template <typename T> DI void lds_st(u32 a, T v) { *(LAS T*)(uintptr_t)a = v; }

DI u32 scan32(u32 v) {
  v += __builtin_amdgcn_update_dpp(0u, v, 0x111, 0xf, 0xf, false);
  v += __builtin_amdgcn_update_dpp(0u, v, 0x112, 0xf, 0xf, false);
  v += __builtin_amdgcn_update_dpp(0u, v, 0x114, 0xf, 0xf, false);
  v += __builtin_amdgcn_update_dpp(0u, v, 0x118, 0xf, 0xf, false);
  v += __builtin_amdgcn_update_dpp(0u, v, 0x142, 0xa, 0xf, false);
  return v;
}
DI u32 scan64(u32 v) {
  v = scan32(v);
  v += __builtin_amdgcn_update_dpp(0u, v, 0x143, 0xc, 0xf, false);
  return v;
}

__global__ __launch_bounds__(K1T) void egnn_bucketize(
    const int* __restrict__ src, const int* __restrict__ dst, const u32x2* __restrict__ eattr,
    const u16* __restrict__ w_e, const u16* __restrict__ w_h, rec12* __restrict__ payload,
    u32* __restrict__ table, u32x4* __restrict__ wfrag, int E, int chunk, u32 mul) {
  __shared__ u32 hist[NBK];
  __shared__ u32 startw[K1T / 64][NBK];
  __shared__ u32x2 st8[CHUNK_MAX];
  __shared__ u32 st4[CHUNK_MAX];
  const int t = threadIdx.x;
  if (blockIdx.x >= NP) {
    const int u = (blockIdx.x - NP) * K1T + t;
    if (u < WF_TOTAL) {
      u16 v[8];
      if (u < WF_WX) {
        const int part = u >> 9, nt = (u >> 8) & 1, s = (u >> 6) & 3, l = u & 63;
        const int k0 = part * 64 + 32 * (l >> 5) + 8 * s, col = nt * 32 + (l & 31);
#pragma unroll
        for (int j = 0; j < 8; ++j) v[j] = w_e[(k0 + j) * 64 + col];
      } else if (u < WF_WH) {
        const int col = u - WF_WX;
#pragma unroll
        for (int j = 0; j < 4; ++j) v[j] = w_e[(129 + j) * 64 + col];
        v[4] = w_e[128 * 64 + col]; v[5] = 0; v[6] = 0; v[7] = 0;
      } else {
        const int x = u - WF_WH, mt = x >> 9, s = (x >> 6) & 7, l = x & 63;
        const int k0 = 16 * s + 8 * (l >> 5), col = mt * 32 + (l & 31);
#pragma unroll
        for (int j = 0; j < 8; ++j) v[j] = w_h[(k0 + j) * 64 + col];
      }
      u32x4 o;
      o.x = v[0] | ((u32)v[1] << 16); o.y = v[2] | ((u32)v[3] << 16);
      o.z = v[4] | ((u32)v[5] << 16); o.w = v[6] | ((u32)v[7] << 16);
      wfrag[u] = o;
    }
    return;
  }
  const int p = blockIdx.x;
  const int ebeg = p * chunk;
  const int n = max(0, min(E - ebeg, chunk));
  const int i0 = 4 * t;
  u32 d[4], s[4]; u32x2 a[4];
  if (i0 + 4 <= n) {
    const u32x4 dv = *(const u32x4*)(dst + ebeg + i0);
    const u32x4 sv = *(const u32x4*)(src + ebeg + i0);
    const u32x4 a0 = *(const u32x4*)(eattr + ebeg + i0);
    const u32x4 a1 = *(const u32x4*)(eattr + ebeg + i0 + 2);
    d[0] = dv.x; d[1] = dv.y; d[2] = dv.z; d[3] = dv.w;
    s[0] = sv.x; s[1] = sv.y; s[2] = sv.z; s[3] = sv.w;
    a[0] = u32x2{a0.x, a0.y}; a[1] = u32x2{a0.z, a0.w}; a[2] = u32x2{a1.x, a1.y}; a[3] = u32x2{a1.z, a1.w};
  } else {
#pragma unroll
    for (int j = 0; j < 4; ++j) {
      const bool ok = i0 + j < n;
      d[j] = ok ? dst[ebeg + i0 + j] : 0; s[j] = ok ? src[ebeg + i0 + j] : 0;
      a[j] = ok ? eattr[ebeg + i0 + j] : u32x2{0, 0};
    }
  }
  if (t < NBK) hist[t] = 0;
  __syncthreads();
  u32 b[4], rk[4];
#pragma unroll
  for (int j = 0; j < 4; ++j) {
    b[j] = (d[j] * mul) >> 16;
    rk[j] = 0;
    if (i0 + j < n) rk[j] = atomicAdd(&hist[b[j]], 1u);
  }
  __syncthreads();
  const int lane = t & 63, wave = t >> 6;
  {
    const u32x4 c = *(const u32x4*)&hist[4 * lane];
    const u32 tot = c.x + c.y + c.z + c.w;
    const u32 ex = scan64(tot) - tot;
    const u32x4 st = u32x4{ex, ex + c.x, ex + c.x + c.y, ex + c.x + c.y + c.z};
    *(u32x4*)&startw[wave][4 * lane] = st;
    if (wave == 0) *(u32x4*)&table[p * NBK + 4 * lane] = st | (c << 16);
  }
#pragma unroll
  for (int j = 0; j < 4; ++j) {
    if (i0 + j < n) {
      const u32 ps = startw[wave][b[j]] + rk[j];
      st8[ps] = a[j];
      st4[ps] = s[j] | (d[j] << 16);
    }
  }
  __syncthreads();
  rec12* out = payload + (size_t)p * chunk;
#pragma unroll
  for (int j = 0; j < 4; ++j) {
    const int i = t + j * K1T;
    if (i < n) {
      const u32x2 x = st8[i];
      rec12 r; r.a = x.x; r.b = x.y; r.c = st4[i];
      out[i] = r;
    }
  }
}

DI void glds16(const void* base, u32 voff, u32 lds_dst) {
  u32 keep;
  asm volatile("s_mov_b32 %0, m0\n\ts_mov_b32 m0, %3\n\ts_nop 0\n\tglobal_load_lds_dwordx4 %1, %2\n\ts_mov_b32 m0, %0"
               : "=&s"(keep) : "v"(voff), "s"(base), "s"(lds_dst) : "memory");
}

__global__ __launch_bounds__(K2T, 2) void egnn_aggregate(
    const u16* __restrict__ h, const u16* __restrict__ pos, const u16* __restrict__ w_x,
    const rec12* __restrict__ payload, const u32* __restrict__ table,
    const u32x4* __restrict__ wfrag, u16* __restrict__ out_h, u16* __restrict__ out_pos,
    int N, int chunk, u32 mul) {
  extern __shared__ __attribute__((aligned(16))) char smem[];
  const u32 LB = (u32)(uintptr_t)(LAS char*)smem;
  const int t = threadIdx.x, lane = t & 63;
  const int wave = __builtin_amdgcn_readfirstlane(t >> 6);
  const int bucket = (blockIdx.x & 7) * (NBK / 8) + (blockIdx.x >> 3);
  const int d0 = (int)(((u32)bucket * 65536u + mul - 1) / mul);
  const int d1 = min(N, (int)(((u32)(bucket + 1) * 65536u + mul - 1) / mul));
  const int nn = d1 - d0;
  if (nn <= 0) return;
  const int r32 = lane & 31, hh = lane >> 5;

  u32 seg[8];
#pragma unroll
  for (int i = 0; i < 8; ++i) seg[i] = table[((t >> 4) + 32 * i) * NBK + bucket];

  for (int i = t; i < (40 * 64 + 40 * 96) / 4; i += K2T) lds_st<f32x4>(LB + L_MACC + 16 * i, f32x4{0, 0, 0, 0});
  if (t < 36) {
    const int row = t >> 2, w = t & 3;
    lds_st<u32>(LB + L_OH + 4 * t, (row >> 1) == w && row < 8 ? (0x3f80u << (16 * (row & 1))) : 0u);
  }
  if (t < NWIN * 32 + 4) lds_st<u32>(LB + L_CNT + 4 * t, 0u);

  if (wave < 4) {
    const int rt = wave >> 1, nt = wave & 1;
    const int node = min(d0 + rt * 32 + r32, N - 1);
    f32x16 acc;
#pragma unroll
    for (int i = 0; i < 16; ++i) acc[i] = 0.f;
#pragma unroll
    for (int s = 0; s < 4; ++s) {
      const u32x4 a = *(const u32x4*)(h + node * 64 + 32 * hh + 8 * s);
      const u32x4 b = wfrag[WF_W1 + (nt * 4 + s) * 64 + lane];
      acc = __builtin_amdgcn_mfma_f32_32x32x16_bf16(__builtin_bit_cast(s16x8, a), __builtin_bit_cast(s16x8, b), acc, 0, 0, 0);
    }
#pragma unroll
    for (int g = 0; g < 4; ++g) {
      const int w = rt * 4 + g;
      if (w < NWIN)
        lds_st<u32x2>(LB + L_ANT + (w * 64 + nt * 32 + r32) * 16 + 8 * hh,
                      u32x2{pack_bf(acc[4 * g], acc[4 * g + 1]), pack_bf(acc[4 * g + 2], acc[4 * g + 3])});
    }
  }

  u32x4 wB[2][4];
#pragma unroll
  for (int nt = 0; nt < 2; ++nt)
#pragma unroll
    for (int s = 0; s < 4; ++s) wB[nt][s] = wfrag[WF_W2 + (nt * 4 + s) * 64 + lane];
  u32x4 wX[2];
  wX[0] = wfrag[WF_WX + r32]; wX[1] = wfrag[WF_WX + 32 + r32];
  const float wx0 = bf_lo(w_x[r32]), wx1 = bf_lo(w_x[32 + r32]);

  const int q4 = (lane >> 2) & 3, p4 = lane & 3, b4 = (lane >> 4) & 1;
  const u32 gbuf = LB + L_POS + wave * 4096;
  const u32 cE = (u32)(b4 * 4 + (p4 ^ hh)) * 16, cO = (u32)(b4 * 4 + (p4 ^ (2 | hh))) * 16;
  u32 aA[4];
#pragma unroll
  for (int s = 0; s < 4; ++s)
    aA[s] = gbuf + (r32 >> 3) * 1024 + (32 * ((r32 >> 2) & 1) + 16 * hh + 4 * (r32 & 3) + (s ^ ((r32 >> 2) & 3))) * 16;
  const u32 nodeKey = (u32)(4 * b4 + p4) * 16;
  const u32 zeroAddr = LB + L_OH + 128;
  const u32 xmask = hh ? 0xffffffffu : 0u;

  u32 maxc = 0;
#pragma unroll
  for (int i = 0; i < 8; ++i) maxc = max(maxc, seg[i] >> 16);

  bool fast = true;
  int nrounds = 1;
  for (int round = 0; round < nrounds; ++round) {
    {
      int t = threadIdx.x;
      asm volatile("" : "+v"(t));
      const int g16 = t >> 4, l16 = t & 15;
      if (!fast) {
#pragma unroll
        for (int i = 0; i < 8; ++i) seg[i] = table[(g16 + 32 * i) * NBK + bucket];
      }
      const int nb16 = (N * 6) >> 4;
      u32x4 pv[8];
#pragma unroll
      for (int i = 0; i < 8; ++i) {
        const int x = t + i * K2T;
        pv[i] = u32x4{0, 0, 0, 0};
        if (x < nb16) pv[i] = ((const u32x4*)pos)[x];
      }
      const int sel = round & 7, base = fast ? 0 : (round >> 3) * 32;
      rec12 rc[16];
      u32 act = 0;
#pragma unroll
      for (int i = 0; i < 8; ++i) {
        const u32 cnt = seg[i] >> 16, st = seg[i] & 0xffffu;
        const rec12* sp = payload + (size_t)(g16 + 32 * i) * chunk + st + base + l16;
        const bool on = fast || i == sel;
#pragma unroll
        for (int k = 0; k < 2; ++k) {
          rc[2 * i + k] = rec12{0, 0, 0};
          if (on && (u32)(base + l16 + 16 * k) < cnt) { rc[2 * i + k] = sp[16 * k]; act |= 1u << (2 * i + k); }
        }
      }
#pragma unroll
      for (int i = 0; i < 8; ++i) {
        const int x = t + i * K2T;
        if (x < nb16) lds_st<u32x4>(LB + L_POS + 16 * x, pv[i]);
      }
      for (int x = nb16 * 8 + t; x < N * 3; x += K2T) lds_st<u16>(LB + L_POS + 2 * x, pos[x]);
      if (round == 0 && maxc > 32) atomicMax((u32*)(smem + L_FLAG), maxc);
      __syncthreads();
      u32 rk[16];
#pragma unroll
      for (int j = 0; j < 16; ++j) {
        rk[j] = 0;
        if (act & (1u << j)) {
          const u32 w = ((rc[j].c >> 16) - d0) >> 3;
          rk[j] = atomicAdd((u32*)(smem + L_CNT) + w * 32 + r32, 1u);
        }
      }
      __syncthreads();
      u32 bas[NWIN], tstart = 0, tsv = 0, cw[NWIN], tw[NWIN];
#pragma unroll
      for (int w = 0; w < NWIN; ++w) {
        const u32 c = lds_ld<u32>(LB + L_CNT + (w * 32 + r32) * 4);
        const u32 inc = scan32(c);
        cw[w] = __builtin_amdgcn_readlane(inc, 31);
        tw[w] = tstart;
        bas[w] = tstart * 32 + inc - c;
        tsv = lane == w ? tstart : tsv;
        tstart += (cw[w] + 31) >> 5;
      }
      tsv = lane == NWIN ? tstart : tsv;
      const int ntiles = tstart;
      if (round == 0 && fast) {
        const u32 fl = __builtin_amdgcn_readfirstlane(lds_ld<u32>(LB + L_FLAG));
        if (fl != 0 || ntiles > CAPT) {
          fast = false;
          nrounds = 8 * (int)((max(fl, 32u) + 31) >> 5);
          __syncthreads();
          if (t < NWIN * 32) lds_st<u32>(LB + L_CNT + 4 * t, 0u);
          round = -1;
          continue;
        }
      }
#pragma unroll
      for (int j = 0; j < 16; ++j) {
        if (act & (1u << j)) {
          const u32 sidx = rc[j].c & 0xffffu, didx = rc[j].c >> 16, dl = didx - d0, w = dl >> 3;
          u32 bs = bas[0];
#pragma unroll
          for (int x = 1; x < NWIN; ++x) bs = w == (u32)x ? bas[x] : bs;
          const u32 ps = bs + rk[j];
          const u32 sa = LB + L_POS + ((sidx * 6) & ~3u), da = LB + L_POS + ((didx * 6) & ~3u);
          const u32 s0 = lds_ld<u32>(sa), s1 = lds_ld<u32>(sa + 4);
          const u32 e0 = lds_ld<u32>(da), e1 = lds_ld<u32>(da + 4);
          const bool so = sidx & 1, dd = didx & 1;
          const float sx = so ? bf_hi(s0) : bf_lo(s0), sy = so ? bf_lo(s1) : bf_hi(s0), sz = so ? bf_hi(s1) : bf_lo(s1);
          const float dx = dd ? bf_hi(e0) : bf_lo(e0), dy = dd ? bf_lo(e1) : bf_hi(e0), dz = dd ? bf_hi(e1) : bf_lo(e1);
          const u32 p0 = pack_bf(1.0f, dx - sx), p1 = pack_bf(dy - sy, dz - sz);
          const float fx = bf_hi(p0), fy = bf_lo(p1), fz = bf_hi(p1);
          const float d2 = round_bf(fx * fx) + round_bf(fy * fy) + round_bf(fz * fz);
          lds_st<u32x4>(LB + L_R1 + ps * 16, u32x4{rc[j].a, rc[j].b, pack_bf(d2, 0.f), (sidx << 7) | ((dl & 7u) << 28)});
          lds_st<u32x2>(LB + L_R2 + ps * 8, u32x2{p0, p1});
        }
      }
      if (t < NWIN * 32) {
        const int w = t >> 5;
        u32 c = cw[0], ts = tw[0];
#pragma unroll
        for (int x = 1; x < NWIN; ++x) { c = w == x ? cw[x] : c; ts = w == x ? tw[x] : ts; }
        const u32 ps = c + r32;
        if (ps < ((c + 31) & ~31u)) lds_st<u32x4>(LB + L_R1 + (ts * 32 + ps) * 16, u32x4{0, 0, 0, 0x80000000u});
      }
      __syncthreads();
      if (t < NWIN * 32) lds_st<u32>(LB + L_CNT + 4 * t, 0u);

      const int order = (wave >> 2) | ((wave & 3) << 1);
      const int t0 = (ntiles * order) >> 3, t1 = (ntiles * (order + 1)) >> 3;
      if (t0 < t1) {
        u32 aD = LB + L_R1 + (t0 * 32 + 4 * hh + q4) * 16 + 12;
        u32 aR = LB + L_R1 + (t0 * 32 + r32) * 16;
        u32 aS = LB + L_R2 + (t0 * 32 + 4 * hh + q4) * 8;
        u32 D[4], Dn[4];
#pragma unroll
        for (int i = 0; i < 4; ++i) D[i] = lds_ld<u32>(aD + 128 * i);
#pragma unroll
        for (int i = 0; i < 4; ++i) glds16(h, (D[i] & 0x00ffff80u) | ((i & 1) ? cO : cE), gbuf + 1024 * i);
        int tile = t0;
        int w = 0;
        while (__builtin_amdgcn_readlane(tsv, w + 1) <= tile) ++w;
        while (tile < t1) {
          const int wend = min(t1, (int)__builtin_amdgcn_readlane(tsv, w + 1));
          u32x4 bX[2];
#pragma unroll
          for (int nt = 0; nt < 2; ++nt) {
            const u32x4 an = lds_ld<u32x4>(LB + L_ANT + (w * 64 + nt * 32 + r32) * 16);
            bX[nt] = hh ? an : wX[nt];
          }
          f32x16 Y[2];
#pragma unroll
          for (int i = 0; i < 16; ++i) { Y[0][i] = 0.f; Y[1][i] = 0.f; }
          for (; tile < wend; ++tile) {
            const u32 dw = lds_ld<u32>(aR + 12);
#pragma unroll
            for (int i = 0; i < 4; ++i) Dn[i] = lds_ld<u32>(aD + 512 + 128 * i);
            u32x4 ax = lds_ld<u32x4>(hh ? LB + L_OH + (dw >> 24) : aR);
            ax.w &= xmask;
            s16x4 S[4];
#pragma unroll
            for (int i = 0; i < 4; ++i) {
              const u32 ad = (D[i] >> 24) == nodeKey ? aS + 64 * i : zeroAddr;
              S[i] = __builtin_amdgcn_ds_read_tr16_b64_v4i16((LAS s16x4*)(uintptr_t)ad);
            }
            asm volatile("s_waitcnt vmcnt(0)" ::: "memory");
            u32x4 A[4];
#pragma unroll
            for (int s = 0; s < 4; ++s) A[s] = lds_ld<u32x4>(aA[s]);
            asm volatile("s_waitcnt lgkmcnt(0)" : "+v"(A[0]), "+v"(A[1]), "+v"(A[2]), "+v"(A[3])::"memory");
            if (tile + 1 < t1) {
#pragma unroll
              for (int i = 0; i < 4; ++i) glds16(h, (Dn[i] & 0x00ffff80u) | ((i & 1) ? cO : cE), gbuf + 1024 * i);
            }
            const s16x8 S0 = __builtin_shufflevector(S[0], S[1], 0, 1, 2, 3, 4, 5, 6, 7);
            const s16x8 S1 = __builtin_shufflevector(S[2], S[3], 0, 1, 2, 3, 4, 5, 6, 7);
#pragma unroll
            for (int nt = 0; nt < 2; ++nt) {
              f32x16 X;
#pragma unroll
              for (int i = 0; i < 16; ++i) X[i] = 0.f;
              X = __builtin_amdgcn_mfma_f32_32x32x16_bf16(__builtin_bit_cast(s16x8, ax), __builtin_bit_cast(s16x8, bX[nt]), X, 0, 0, 0);
#pragma unroll
              for (int s = 0; s < 4; ++s)
                X = __builtin_amdgcn_mfma_f32_32x32x16_bf16(__builtin_bit_cast(s16x8, A[s]), __builtin_bit_cast(s16x8, wB[nt][s]), X, 0, 0, 0);
              u32x4 m0, m1;
#pragma unroll
              for (int i = 0; i < 4; ++i) {
                m0[i] = pack_bf(silu_f(X[2 * i]), silu_f(X[2 * i + 1]));
                m1[i] = pack_bf(silu_f(X[8 + 2 * i]), silu_f(X[8 + 2 * i + 1]));
              }
              Y[nt] = __builtin_amdgcn_mfma_f32_32x32x16_bf16(S0, __builtin_bit_cast(s16x8, m0), Y[nt], 0, 0, 0);
              Y[nt] = __builtin_amdgcn_mfma_f32_32x32x16_bf16(S1, __builtin_bit_cast(s16x8, m1), Y[nt], 0, 0, 0);
            }
#pragma unroll
            for (int i = 0; i < 4; ++i) D[i] = Dn[i];
            aD += 512; aR += 512; aS += 256;
          }
#pragma unroll
          for (int g = 0; g < 4; ++g) {
            const int node = w * 8 + 2 * g + hh;
            float* ma = (float*)(smem + L_MACC) + node * 64 + r32;
            atomicAdd(ma, Y[0][4 * g]);
            atomicAdd(ma + 32, Y[1][4 * g]);
            float* pa = (float*)(smem + L_PACC) + node * 96 + r32;
#pragma unroll
            for (int c = 1; c < 4; ++c) atomicAdd(pa + (c - 1) * 32, Y[0][4 * g + c] * wx0 + Y[1][4 * g + c] * wx1);
          }
          ++w;
        }
      }
      __syncthreads();
    }
  }

  if (wave < 4) {
    const int ct = wave >> 1, mt = wave & 1;
    const int nl = ct * 32 + r32;
    const int node = min(d0 + nl, N - 1);
    f32x16 acc;
#pragma unroll
    for (int i = 0; i < 16; ++i) acc[i] = 0.f;
#pragma unroll
    for (int s = 0; s < 8; ++s) {
      const u32x4 a = wfrag[WF_WH + (mt * 8 + s) * 64 + lane];
      u32x4 b;
      if (s < 4) {
        b = *(const u32x4*)(h + node * 64 + 16 * s + 8 * hh);
      } else {
        const u32 ad = LB + L_MACC + (min(nl, 39) * 64 + 16 * (s - 4) + 8 * hh) * 4;
        const f32x4 x = lds_ld<f32x4>(ad), y = lds_ld<f32x4>(ad + 16);
        b = u32x4{pack_bf(x.x, x.y), pack_bf(x.z, x.w), pack_bf(y.x, y.y), pack_bf(y.z, y.w)};
      }
      acc = __builtin_amdgcn_mfma_f32_32x32x16_bf16(__builtin_bit_cast(s16x8, a), __builtin_bit_cast(s16x8, b), acc, 0, 0, 0);
    }
    if (nl < nn) {
#pragma unroll
      for (int g = 0; g < 4; ++g) {
        const int f = mt * 32 + 8 * g + 4 * hh;
        const u32x2 hv = *(const u32x2*)(h + node * 64 + f);
        const float v0 = bf_lo(hv.x) + round_bf(silu_f(round_bf(acc[4 * g])));
        const float v1 = bf_hi(hv.x) + round_bf(silu_f(round_bf(acc[4 * g + 1])));
        const float v2 = bf_lo(hv.y) + round_bf(silu_f(round_bf(acc[4 * g + 2])));
        const float v3 = bf_hi(hv.y) + round_bf(silu_f(round_bf(acc[4 * g + 3])));
        *(u32x2*)(out_h + node * 64 + f) = u32x2{pack_bf(v0, v1), pack_bf(v2, v3)};
      }
    }
  } else {
    const int x = t - 256, item = x >> 1, half = x & 1;
    float sum = 0.f;
    if (item < nn * 3) {
#pragma unroll
      for (int i = 0; i < 4; ++i) {
        const f32x4 v = lds_ld<f32x4>(LB + L_PACC + (item * 32 + half * 16 + 4 * i) * 4);
        sum += (v.x + v.y) + (v.z + v.w);
      }
    }
    sum += __shfl_xor(sum, 1);
    if (item < nn * 3 && half == 0) {
      const float pv = bf_lo(pos[d0 * 3 + item]);
      out_pos[d0 * 3 + item] = (u16)(pack_bf(pv + round_bf(sum), 0.f) & 0xffffu);
    }
  }
}

extern "C" void kernel_launch(void* const* d_in, const int* in_sizes, int n_in, void* d_out, int out_size,
                              void* d_ws, size_t ws_size, hipStream_t stream) {
  const u16* h = (const u16*)d_in[0];
  const u16* pos = (const u16*)d_in[1];
  const u32x2* eattr = (const u32x2*)d_in[2];
  const int* src = (const int*)d_in[3];
  const int* dst = (const int*)d_in[4];
  const u16* w_e = (const u16*)d_in[5];
  const u16* w_x = (const u16*)d_in[6];
  const u16* w_h = (const u16*)d_in[7];
  const int N = in_sizes[0] / 64;
  const int E = in_sizes[3];
  const int chunk = ((E + NP - 1) / NP + 3) & ~3;
  if (N > NMAX || N <= 0 || chunk > CHUNK_MAX) return;
  u32 mul = (u32)((65536ull * NBK) / (unsigned)N);
  if (mul > 65536u) mul = 65536u;
  char* ws = (char*)d_ws;
  u32x4* wfrag = (u32x4*)ws;
  u32* table = (u32*)(ws + 65536);
  rec12* payload = (rec12*)(ws + 65536 + NP * NBK * 4);
  u16* out_h = (u16*)d_out;
  u16* out_pos = out_h + (size_t)N * 64;
  (void)hipFuncSetAttribute((const void*)egnn_aggregate, hipFuncAttributeMaxDynamicSharedMemorySize, L_TOTAL);
  egnn_bucketize<<<NP + WBLK, K1T, 0, stream>>>(src, dst, eattr, w_e, w_h, payload, table, wfrag, E, chunk, mul);
  egnn_aggregate<<<NBK, K2T, L_TOTAL, stream>>>(h, pos, w_x, payload, table, wfrag, out_h, out_pos, N, chunk, mul);
}
```

```cpp
#include <hip/hip_runtime.h>
#include <hip/hip_bf16.h>
#include <stdint.h>

typedef unsigned int u32;
typedef unsigned short u16;
typedef unsigned long long u64;
typedef __attribute__((ext_vector_type(16))) u32 u32x16;
typedef __attribute__((ext_vector_type(8))) u32 u32x8;
typedef __attribute__((ext_vector_type(4))) u32 u32x4;
typedef __attribute__((ext_vector_type(3))) u32 u32x3;
typedef __attribute__((ext_vector_type(2))) u32 u32x2;
typedef __attribute__((ext_vector_type(8))) short s16x8;
typedef __attribute__((ext_vector_type(4))) short s16x4;
typedef __attribute__((ext_vector_type(16))) float f32x16;
typedef __attribute__((ext_vector_type(4))) float f32x4;
typedef __attribute__((ext_vector_type(2))) float f32x2;
typedef __attribute__((ext_vector_type(2))) __bf16 bf16x2;
#define LAS __attribute__((address_space(3)))
#define GAS __attribute__((address_space(1)))
#define DI __device__ __forceinline__
#define SCHED_FENCE() __builtin_amdgcn_sched_barrier(0)

#define TILE_LOOP_ASM \
  "s_mov_b32 s96, m0\n\t" \
  "s_mov_b32 m0, %[gbuf]\n\t" \
  "s_movk_i32 s97, 0xc00\n\t" \
  "s_movk_i32 s98, 0x800\n\t" \
  "s_movk_i32 s99, 0x400\n\t" \
  "s_lshl_b32 s90, %[tile], 9\n\t" \
  "s_lshl_b32 s91, %[tile], 8\n\t" \
  "v_add_u32 v85, s90, v88\n\t" \
  "v_add_u32 v83, s91, v90\n\t" \
  "v_add_u32 v84, v85, v89\n\t" \
  "v_mov_b64 v[160:161], 0\n\t" \
  "v_mov_b64 v[162:163], 0\n\t" \
  "v_mov_b64 v[164:165], 0\n\t" \
  "v_mov_b64 v[166:167], 0\n\t" \
  "v_mov_b64 v[168:169], 0\n\t" \
  "v_mov_b64 v[170:171], 0\n\t" \
  "v_mov_b64 v[172:173], 0\n\t" \
  "v_mov_b64 v[174:175], 0\n\t" \
  "v_mov_b64 v[176:177], 0\n\t" \
  "v_mov_b64 v[178:179], 0\n\t" \
  "v_mov_b64 v[180:181], 0\n\t" \
  "v_mov_b64 v[182:183], 0\n\t" \
  "v_mov_b64 v[184:185], 0\n\t" \
  "v_mov_b64 v[186:187], 0\n\t" \
  "v_mov_b64 v[188:189], 0\n\t" \
  "v_mov_b64 v[190:191], 0\n\t" \
  ".Lloop%=:\n\t" \
  "v_mfma_f32_32x32x16_bf16 v[208:223], v[240:243], v[248:251], 0\n\t" \
  "v_cvt_pk_bf16_f32 v124, v104, v105\n\t" \
  "v_cvt_pk_bf16_f32 v125, v106, v107\n\t" \
  "v_exp_f32 v104, v200\n\t" \
  "s_mov_b32 exec_lo, 0\n\t" \
  "ds_read_u16 v84, v84 offset:14\n\t" \
  "s_mov_b32 exec_lo, -1\n\t" \
  "v_exp_f32 v105, v201\n\t" \
  "ds_read_u16 v96, v85 offset:522\n\t" \
  "ds_read_u16 v98, v85 offset:650\n\t" \
  "v_mfma_f32_32x32x16_bf16 v[208:223], v[232:235], v[128:131], v[208:223]\n\t" \
  "v_cvt_pk_bf16_f32 v126, v108, v109\n\t" \
  "v_cvt_pk_bf16_f32 v127, v110, v111\n\t" \
  "v_exp_f32 v106, v202\n\t" \
  "v_exp_f32 v107, v203\n\t" \
  "ds_read_u16 v100, v85 offset:778\n\t" \
  "ds_read_u16 v102, v85 offset:906\n\t" \
  "v_mfma_f32_32x32x16_bf16 v[208:223], v[236:239], v[132:135], v[208:223]\n\t" \
  "v_cvt_pk_bf16_f32 v120, v48, v49\n\t" \
  "v_cvt_pk_bf16_f32 v121, v50, v51\n\t" \
  "v_exp_f32 v108, v204\n\t" \
  "v_exp_f32 v109, v205\n\t" \
  "s_add_i32 %[tile], %[tile], 1\n\t" \
  "s_cmp_eq_u32 %[tile], %[tend]\n\t" \
  "s_cbranch_scc1 .Lsw%=\n\t" \
  ".Lswback%=:\n\t" \
  "v_mfma_f32_32x32x16_bf16 v[176:191], v[124:127], v[116:119], v[176:191]\n\t" \
  "v_cvt_pk_bf16_f32 v122, v52, v53\n\t" \
  "v_cvt_pk_bf16_f32 v123, v54, v55\n\t" \
  "v_exp_f32 v110, v206\n\t" \
  "v_exp_f32 v111, v207\n\t" \
  "v_exp_f32 v48, v192\n\t" \
  "s_waitcnt vmcnt(0) lgkmcnt(0)\n\t" \
  "ds_read_b128 v[240:243], v84\n\t" \
  "ds_read_b128 v[224:227], v92\n\t" \
  "ds_read_b128 v[228:231], v93\n\t" \
  "ds_read_b128 v[232:235], v94\n\t" \
  "ds_read_b128 v[236:239], v95\n\t" \
  "v_exp_f32 v49, v193\n\t" \
  "v_mfma_f32_32x32x16_bf16 v[176:191], v[120:123], v[112:115], v[176:191]\n\t" \
  "v_exp_f32 v50, v194\n\t" \
  "v_exp_f32 v51, v195\n\t" \
  "v_exp_f32 v52, v196\n\t" \
  "v_exp_f32 v53, v197\n\t" \
  "v_exp_f32 v54, v198\n\t" \
  "v_exp_f32 v55, v199\n\t" \
  "v_pk_add_f32 v[104:105], v[104:105], 1.0 op_sel_hi:[1,0]\n\t" \
  "v_pk_add_f32 v[106:107], v[106:107], 1.0 op_sel_hi:[1,0]\n\t" \
  "v_pk_add_f32 v[108:109], v[108:109], 1.0 op_sel_hi:[1,0]\n\t" \
  "v_pk_add_f32 v[110:111], v[110:111], 1.0 op_sel_hi:[1,0]\n\t" \
  "v_pk_add_f32 v[48:49], v[48:49], 1.0 op_sel_hi:[1,0]\n\t" \
  "v_pk_add_f32 v[50:51], v[50:51], 1.0 op_sel_hi:[1,0]\n\t" \
  "v_pk_add_f32 v[52:53], v[52:53], 1.0 op_sel_hi:[1,0]\n\t" \
  "v_pk_add_f32 v[54:55], v[54:55], 1.0 op_sel_hi:[1,0]\n\t" \
  "v_rcp_f32 v104, v104\n\t" \
  "v_bitop3_b32 v44, v252, v91, v83 bitop3:0x6a\n\t" \
  "s_cmp_ge_i32 %[tile], %[tlast]\n\t" \
  "s_waitcnt lgkmcnt(0)\n\t" \
  "s_cbranch_scc1 .Lskip%=\n\t" \
  "buffer_load_dwordx4 v[96:97], %[rs], s97 idxen offen offset:0 sc1 lds\n\t" \
  "buffer_load_dwordx4 v[98:99], %[rs], s98 idxen offen offset:1024 sc1 lds\n\t" \
  "buffer_load_dwordx4 v[100:101], %[rs], s99 idxen offen offset:2048 sc1 lds\n\t" \
  "buffer_load_dwordx4 v[102:103], %[rs], 0 idxen offen offset:3072 sc1 lds\n\t" \
  ".Lskip%=:\n\t" \
  "v_rcp_f32 v105, v105\n\t" \
  "v_bitop3_b32 v45, v253, v91, v83 bitop3:0x6a\n\t" \
  "v_rcp_f32 v106, v106\n\t" \
  "v_bitop3_b32 v46, v254, v91, v83 bitop3:0x6a\n\t" \
  "v_rcp_f32 v107, v107\n\t" \
  "v_bitop3_b32 v47, v255, v91, v83 bitop3:0x6a\n\t" \
  "v_rcp_f32 v108, v108\n\t" \
  "ds_read_b64_tr_b16 v[112:113], v44 offset:0\n\t" \
  "ds_read_b64_tr_b16 v[114:115], v45 offset:64\n\t" \
  "ds_read_b64_tr_b16 v[116:117], v46 offset:128\n\t" \
  "ds_read_b64_tr_b16 v[118:119], v47 offset:192\n\t" \
  "v_rcp_f32 v109, v109\n\t" \
  "ds_read2_b32 v[252:253], v85 offset0:3 offset1:35\n\t" \
  "ds_read2_b32 v[254:255], v85 offset0:67 offset1:99\n\t" \
  "v_rcp_f32 v110, v110\n\t" \
  "v_rcp_f32 v111, v111\n\t" \
  "v_rcp_f32 v48, v48\n\t" \
  "v_rcp_f32 v49, v49\n\t" \
  "v_rcp_f32 v50, v50\n\t" \
  "v_rcp_f32 v51, v51\n\t" \
  "v_rcp_f32 v52, v52\n\t" \
  "v_rcp_f32 v53, v53\n\t" \
  "v_rcp_f32 v54, v54\n\t" \
  "v_rcp_f32 v55, v55\n\t" \
  "v_pk_mul_f32 v[104:105], v[200:201], v[104:105]\n\t" \
  "v_pk_mul_f32 v[106:107], v[202:203], v[106:107]\n\t" \
  "v_pk_mul_f32 v[108:109], v[204:205], v[108:109]\n\t" \
  "v_pk_mul_f32 v[110:111], v[206:207], v[110:111]\n\t" \
  "v_pk_mul_f32 v[48:49], v[192:193], v[48:49]\n\t" \
  "v_pk_mul_f32 v[50:51], v[194:195], v[50:51]\n\t" \
  "v_pk_mul_f32 v[52:53], v[196:197], v[52:53]\n\t" \
  "v_pk_mul_f32 v[54:55], v[198:199], v[54:55]\n\t" \
  "v_mfma_f32_32x32x16_bf16 v[192:207], v[240:243], v[244:247], 0\n\t" \
  "v_cvt_pk_bf16_f32 v124, v104, v105\n\t" \
  "v_cvt_pk_bf16_f32 v125, v106, v107\n\t" \
  "v_exp_f32 v104, v216\n\t" \
  "v_exp_f32 v105, v217\n\t" \
  "v_mfma_f32_32x32x16_bf16 v[192:207], v[224:227], v[128:131], v[192:207]\n\t" \
  "v_cvt_pk_bf16_f32 v126, v108, v109\n\t" \
  "v_cvt_pk_bf16_f32 v127, v110, v111\n\t" \
  "v_exp_f32 v106, v218\n\t" \
  "v_exp_f32 v107, v219\n\t" \
  "s_cmp_eq_u32 %[tile], %[tprio]\n\t" \
  "s_cbranch_scc1 .Lpr%=\n\t" \
  ".Lprback%=:\n\t" \
  "v_mfma_f32_32x32x16_bf16 v[192:207], v[228:231], v[132:135], v[192:207]\n\t" \
  "v_cvt_pk_bf16_f32 v120, v48, v49\n\t" \
  "v_cvt_pk_bf16_f32 v121, v50, v51\n\t" \
  "v_exp_f32 v108, v220\n\t" \
  "v_exp_f32 v109, v221\n\t" \
  "s_waitcnt lgkmcnt(0)\n\t" \
  "v_mfma_f32_32x32x16_bf16 v[160:175], v[124:127], v[116:119], v[160:175]\n\t" \
  "v_cvt_pk_bf16_f32 v122, v52, v53\n\t" \
  "v_cvt_pk_bf16_f32 v123, v54, v55\n\t" \
  "v_exp_f32 v110, v222\n\t" \
  "v_exp_f32 v111, v223\n\t" \
  "v_exp_f32 v48, v208\n\t" \
  "v_exp_f32 v49, v209\n\t" \
  "v_mfma_f32_32x32x16_bf16 v[160:175], v[120:123], v[112:115], v[160:175]\n\t" \
  "v_exp_f32 v50, v210\n\t" \
  "v_exp_f32 v51, v211\n\t" \
  "v_exp_f32 v52, v212\n\t" \
  "v_exp_f32 v53, v213\n\t" \
  "v_exp_f32 v54, v214\n\t" \
  "v_exp_f32 v55, v215\n\t" \
  "v_pk_add_f32 v[104:105], v[104:105], 1.0 op_sel_hi:[1,0]\n\t" \
  "v_pk_add_f32 v[106:107], v[106:107], 1.0 op_sel_hi:[1,0]\n\t" \
  "v_pk_add_f32 v[108:109], v[108:109], 1.0 op_sel_hi:[1,0]\n\t" \
  "v_pk_add_f32 v[110:111], v[110:111], 1.0 op_sel_hi:[1,0]\n\t" \
  "v_pk_add_f32 v[48:49], v[48:49], 1.0 op_sel_hi:[1,0]\n\t" \
  "v_pk_add_f32 v[50:51], v[50:51], 1.0 op_sel_hi:[1,0]\n\t" \
  "v_pk_add_f32 v[52:53], v[52:53], 1.0 op_sel_hi:[1,0]\n\t" \
  "v_pk_add_f32 v[54:55], v[54:55], 1.0 op_sel_hi:[1,0]\n\t" \
  "v_rcp_f32 v104, v104\n\t" \
  "v_add_u32 v85, 0x200, v85\n\t" \
  "v_rcp_f32 v105, v105\n\t" \
  "v_add_u32 v83, 0x100, v83\n\t" \
  "v_rcp_f32 v106, v106\n\t" \
  "v_add_u32 v84, v85, v89\n\t" \
  "v_rcp_f32 v107, v107\n\t" \
  "v_rcp_f32 v108, v108\n\t" \
  "v_rcp_f32 v109, v109\n\t" \
  "v_rcp_f32 v110, v110\n\t" \
  "s_cmp_lt_i32 %[tile], %[tend]\n\t" \
  "v_rcp_f32 v111, v111\n\t" \
  "v_rcp_f32 v48, v48\n\t" \
  "v_rcp_f32 v49, v49\n\t" \
  "v_rcp_f32 v50, v50\n\t" \
  "v_rcp_f32 v51, v51\n\t" \
  "v_rcp_f32 v52, v52\n\t" \
  "v_rcp_f32 v53, v53\n\t" \
  "v_rcp_f32 v54, v54\n\t" \
  "v_rcp_f32 v55, v55\n\t" \
  "v_pk_mul_f32 v[104:105], v[216:217], v[104:105]\n\t" \
  "v_pk_mul_f32 v[106:107], v[218:219], v[106:107]\n\t" \
  "v_pk_mul_f32 v[108:109], v[220:221], v[108:109]\n\t" \
  "v_pk_mul_f32 v[110:111], v[222:223], v[110:111]\n\t" \
  "v_pk_mul_f32 v[48:49], v[208:209], v[48:49]\n\t" \
  "v_pk_mul_f32 v[50:51], v[210:211], v[50:51]\n\t" \
  "v_pk_mul_f32 v[52:53], v[212:213], v[52:53]\n\t" \
  "v_pk_mul_f32 v[54:55], v[214:215], v[54:55]\n\t" \
  "s_cbranch_scc1 .Lloop%=\n\t" \
  "s_mov_b32 m0, s96\n\t" \
  "ds_read_b128 v[208:211], v80 offset:0\n\t" \
  "ds_read_b128 v[212:215], v80 offset:32\n\t" \
  "ds_read_b128 v[216:219], v80 offset:64\n\t" \
  "ds_read_b128 v[220:223], v80 offset:96\n\t" \
  "v_cvt_pk_bf16_f32 v120, v48, v49\n\t" \
  "v_cvt_pk_bf16_f32 v121, v50, v51\n\t" \
  "v_cvt_pk_bf16_f32 v122, v52, v53\n\t" \
  "v_cvt_pk_bf16_f32 v123, v54, v55\n\t" \
  "v_cvt_pk_bf16_f32 v124, v104, v105\n\t" \
  "v_cvt_pk_bf16_f32 v125, v106, v107\n\t" \
  "v_cvt_pk_bf16_f32 v126, v108, v109\n\t" \
  "v_cvt_pk_bf16_f32 v127, v110, v111\n\t" \
  "v_mfma_f32_32x32x16_bf16 v[176:191], v[120:123], v[112:115], v[176:191]\n\t" \
  "v_add_u32 v85, %[slab], v81\n\t" \
  "v_mfma_f32_32x32x16_bf16 v[176:191], v[124:127], v[116:119], v[176:191]\n\t" \
  "v_add_u32 v84, %[slab], v82\n\t" \
  "ds_read_b128 v[104:107], v80 offset:128\n\t" \
  "ds_read_b128 v[108:111], v80 offset:160\n\t" \
  "ds_read_b128 v[48:51], v80 offset:192\n\t" \
  "ds_read_b128 v[52:55], v80 offset:224\n\t" \
  "s_mov_b32 exec_lo, 0x11111111\n\t" \
  "s_mov_b32 exec_hi, 0x11111111\n\t" \
  "ds_write_b128 v85, v[160:163] offset:0\n\t" \
  "ds_write_b128 v85, v[164:167] offset:32\n\t" \
  "ds_write_b128 v85, v[168:171] offset:64\n\t" \
  "ds_write_b128 v85, v[172:175] offset:96\n\t" \
  "s_nop 3\n\t" \
  "ds_write_b128 v85, v[176:179] offset:128\n\t" \
  "ds_write_b128 v85, v[180:183] offset:160\n\t" \
  "ds_write_b128 v85, v[184:187] offset:192\n\t" \
  "ds_write_b128 v85, v[188:191] offset:224\n\t" \
  "s_mov_b64 exec, -1\n\t" \
  "v_mov_b64 v[112:113], 0\n\t" \
  "v_mov_b64 v[114:115], 0\n\t" \
  "v_mov_b64 v[116:117], 0\n\t" \
  "v_mov_b64 v[118:119], 0\n\t" \
  "s_waitcnt lgkmcnt(8)\n\t" \
  "v_pk_mul_f32 v[44:45], v[160:161], v[208:209]\n\t" \
  "v_pk_mul_f32 v[46:47], v[162:163], v[210:211]\n\t" \
  "v_pk_mul_f32 v[120:121], v[164:165], v[212:213]\n\t" \
  "v_pk_mul_f32 v[122:123], v[166:167], v[214:215]\n\t" \
  "v_pk_fma_f32 v[44:45], v[168:169], v[216:217], v[44:45]\n\t" \
  "v_pk_fma_f32 v[46:47], v[170:171], v[218:219], v[46:47]\n\t" \
  "v_pk_fma_f32 v[120:121], v[172:173], v[220:221], v[120:121]\n\t" \
  "v_pk_fma_f32 v[122:123], v[174:175], v[222:223], v[122:123]\n\t" \
  "v_pk_fma_f32 v[44:45], v[176:177], v[104:105], v[44:45]\n\t" \
  "v_pk_fma_f32 v[46:47], v[178:179], v[106:107], v[46:47]\n\t" \
  "v_pk_fma_f32 v[120:121], v[180:181], v[108:109], v[120:121]\n\t" \
  "v_pk_fma_f32 v[122:123], v[182:183], v[110:111], v[122:123]\n\t" \
  "v_pk_fma_f32 v[44:45], v[184:185], v[48:49], v[44:45]\n\t" \
  "v_pk_fma_f32 v[46:47], v[186:187], v[50:51], v[46:47]\n\t" \
  "v_pk_fma_f32 v[120:121], v[188:189], v[52:53], v[120:121]\n\t" \
  "v_pk_fma_f32 v[122:123], v[190:191], v[54:55], v[122:123]\n\t" \
  "v_pk_add_f32 v[44:45], v[44:45], v[120:121]\n\t" \
  "v_pk_add_f32 v[46:47], v[46:47], v[122:123]\n\t" \
  "s_nop 0\n\t" \
  "v_pk_add_f32 v[44:45], v[44:45], v[46:47]\n\t" \
  "s_nop 0\n\t" \
  "v_add_f32 v44, v44, v45\n\t" \
  "ds_write_b32 v84, v44\n\t" \
  "s_waitcnt lgkmcnt(0)\n\t" \
  "s_branch .Lend%=\n\t" \
  ".Lsw%=:\n\t" \
  "ds_read_b128 v[244:247], v86\n\t" \
  "s_branch .Lswback%=\n\t" \
  ".Lpr%=:\n\t" \
  "s_setprio 0\n\t" \
  "s_branch .Lprback%=\n\t" \
  ".Lend%=:\n\t" \
  ""
#define TILE_LOOP_CLOBBERS "v44", "v45", "v46", "v47", "v83", "v84", "v85", "v120", "v121", "v122", "v123", "v124", "v125", "v126", "v127", "v160", "v161", "v162", "v163", "v164", "v165", "v166", "v167", "v168", "v169", "v170", "v171", "v172", "v173", "v174", "v175", "v176", "v177", "v178", "v179", "v180", "v181", "v182", "v183", "v184", "v185", "v186", "v187", "v188", "v189", "v190", "v191", "v208", "v209", "v210", "v211", "v212", "v213", "v214", "v215", "v216", "v217", "v218", "v219", "v220", "v221", "v222", "v223", "s90", "s91", "s96", "s97", "s98", "s99", "scc", "memory"

constexpr int NT = 512;
constexpr int NWG_MAX = 256;
constexpr int CHUNK_MAX = 2560;
constexpr int NWIN = 5;
constexpr int CAPT = 91;
constexpr int CAP = CAPT * 32;
constexpr int SLOTS = 6;
constexpr int NMAX = 10064;
constexpr u32 SPIN_MAX = 1u << 22;

constexpr int L_OH = 0;
constexpr int L_SYNC = 256;
constexpr int L_CLK = 320;
constexpr int L_PD = 384;
constexpr int L_WX = L_PD + 640;
constexpr int L_PLAN = L_WX + 256;
constexpr int L_TOT = L_PLAN + 192;
constexpr int L_DUMP = L_TOT + 64;
constexpr int L_HROW = L_DUMP + 32;
constexpr int L_WXF = L_HROW + 5120;
constexpr int L_R1 = L_WXF + 1024;
constexpr int L_R2 = L_R1 + CAP * 16;
constexpr int L_POS = L_R2 + CAP * 8;
constexpr int POS_BYTES = 60416;
constexpr int L_SLAB = L_POS + 8 * 4096;
constexpr int SLAB_BYTES = 2304;
constexpr int L_W = L_POS + POS_BYTES;
constexpr int L_ANT = L_W + 128 * 128;
constexpr int L_ADJ = L_ANT + NWIN * 1024;
constexpr int L_TOTAL = L_ADJ + 4096;
static_assert(L_TOTAL <= 163840, "LDS budget");
static_assert(L_SLAB + 12 * SLAB_BYTES <= L_POS + POS_BYTES, "slabs");
static_assert(L_R2 < 65536, "offset fields");
constexpr int L_STG = L_R1;
constexpr int L_HIST = L_STG + CHUNK_MAX * 16;
constexpr int L_START = L_HIST + 8192;
constexpr int L_IDX = L_START + 8192;
static_assert(L_IDX + CAP * 4 <= L_POS, "early aliases");
constexpr float NEG_LOG2E = -1.4426950408889634f, NEG_LN2 = -0.6931471805599453f;
constexpr u32 TAG_HI_MASK = 0xfffffc0fu;
enum { SY_HIST, SY_START, SY_STG, SY_W, SY_TOT, SY_LIST, SY_EPOCH, SY_BND, SY_B = 10, SY_F = 11, SY_PAIR = 12 };

constexpr u32 WS_EPOCH = 0;
constexpr u32 WS_FLAG = 4096;
constexpr u32 WS_B = 65536;
constexpr u32 WS_GRAN = WS_B + 40 * NWG_MAX * 128;
constexpr u32 WS_PAY = WS_GRAN + NWG_MAX * NWG_MAX * 16;
constexpr u32 WS_HAND = WS_PAY + 8 * NWG_MAX * 4096 * 16;

DI float bf_lo(u32 v) { return __uint_as_float(v << 16); }
DI float bf_hi(u32 v) { return __uint_as_float(v & 0xffff0000u); }
DI u32 cvt_pk(float a, float b) { return __builtin_bit_cast(u32, __builtin_convertvector((f32x2{a, b}), bf16x2)); }
DI float round_bf(float a) { return (float)(__bf16)a; }
DI float silu_f(float x) {
  return x * __builtin_amdgcn_rcpf(1.0f + __builtin_amdgcn_exp2f(x * -1.4426950408889634f));
}
template <typename T> DI T lds_ld(u32 a) { return *(const LAS T*)(uintptr_t)a; }
template <typename T> DI void lds_st(u32 a, T v) { *(LAS T*)(uintptr_t)a = v; }
DI u32x2 lds_tr(u32 a) { return __builtin_bit_cast(u32x2, __builtin_amdgcn_ds_read_tr16_b64_v4i16((LAS s16x4*)(uintptr_t)a)); }
DI u32x4 lds_tr2(u32 a) { const u32x2 x = lds_tr(a), y = lds_tr(a + 512); return u32x4{x.x, x.y, y.x, y.y}; }
DI s16x8 as8(u32x4 a) { return __builtin_bit_cast(s16x8, a); }
DI u32x4 sub4(u32x16 v, int k) { return u32x4{v[4 * k], v[4 * k + 1], v[4 * k + 2], v[4 * k + 3]}; }
#define MFMA(a, b, c) __builtin_amdgcn_mfma_f32_32x32x16_bf16(as8(a), as8(b), c, 0, 0, 0)

DI u32 scan64(u32 v) {
  v += __builtin_amdgcn_update_dpp(0u, v, 0x111, 0xf, 0xf, true);
  v += __builtin_amdgcn_update_dpp(0u, v, 0x112, 0xf, 0xf, true);
  v += __builtin_amdgcn_update_dpp(0u, v, 0x114, 0xf, 0xf, true);
  v += __builtin_amdgcn_update_dpp(0u, v, 0x118, 0xf, 0xf, true);
  v += __builtin_amdgcn_update_dpp(0u, v, 0x142, 0xa, 0xf, false);
  v += __builtin_amdgcn_update_dpp(0u, v, 0x143, 0xc, 0xf, false);
  return v;
}

DI int fresh_lane() {
  int l;
  asm volatile("v_mbcnt_lo_u32_b32 %0, -1, 0\n\tv_mbcnt_hi_u32_b32 %0, -1, %0" : "=v"(l));
  return l;
}

DI void unpack_bounds(u32x4 g, u32 (&sb)[6]) {
  sb[0] = g.x & 0xfff; sb[1] = (g.x >> 12) & 0xfff; sb[2] = __builtin_amdgcn_alignbit(g.y, g.x, 24) & 0xfff;
  sb[3] = g.z & 0xfff; sb[4] = (g.z >> 12) & 0xfff; sb[5] = __builtin_amdgcn_alignbit(g.w, g.z, 24) & 0xfff;
}

constexpr int HI_SHARE = 46;
constexpr int CUT_MIN = 12;
constexpr int PRIO_A = 110, PRIO_B = 26;
constexpr int UNGATED = 5;
constexpr int CODE_LINES = 215;
#define STORE_PAD() do { SCHED_FENCE(); asm volatile("s_nop 1"); SCHED_FENCE(); } while (0)

DI void sy_arrive(int i) {
  u32 a = L_SYNC + 4 * i, one = 1;
  asm volatile("s_mov_b64 exec, 1\n\tds_add_u32 %0, %1\n\ts_mov_b64 exec, -1" ::"v"(a), "v"(one) : "memory");
}
DI void sy_wait(int i, u32 n) {
  u32 a = L_SYNC + 4 * i, v, s;
  asm volatile(
      ".Lw%=:\n\tds_read_b32 %[v], %[a]\n\ts_waitcnt lgkmcnt(0)\n\tv_readfirstlane_b32 %[s], %[v]\n\t"
      "s_cmp_lt_u32 %[s], %[n]\n\ts_cbranch_scc1 .Lw%="
      : [v] "=&v"(v), [s] "=&s"(s) : [a] "v"(a), [n] "s"(n) : "memory", "scc");
}

struct Ctx {
  const u16* h; const u16* pos; const u16* w_h; u16* out_h; u16* out_pos;
  __amdgpu_buffer_rsrc_t hand;
  int N, d0, nn, wave;
  u32 tagA, tagB;
  u32 tagLo, tagHi;
};
DI void make_tags(Ctx& c, u32 lo, u32 hi) {
  c.tagA = (lo + 1u) & 0x0fffffffu;
  c.tagB = ((lo >> 4) + hi + lo * 0x9e3779b1u + 0x05a17c3bu) & 0x0fffffffu;
  c.tagLo = (lo + 1u) << 16;
  c.tagHi = ((c.tagB << 10) | (c.tagB >> 24)) & TAG_HI_MASK;
}

DI void slow_bucket(const Ctx& c, int bucket, int nwg) {
  constexpr int S_A = L_R1, S_ACC = S_A + 40 * 256, ROW = 68;
  static_assert(S_ACC + 8 * 40 * ROW * 4 <= L_W, "fallback tables");
  const int wave = c.wave, lane = fresh_lane(), t = wave * 64 + lane;
  {
    const u32 foff = t < nwg ? WS_FLAG + 8 * t : 0xfffffff0u;
    for (u32 spins = 0; spins < SPIN_MAX; ++spins) {
      asm volatile("" ::: "memory");
      const u32x2 fl = __builtin_amdgcn_raw_buffer_load_b64(c.hand, foff, 0, 16);
      if (__builtin_amdgcn_ballot_w64(t < nwg && (fl.x != c.tagA || fl.y != c.tagB)) == 0) break;
    }
  }
  __syncthreads();
  for (int i = t; i < 40 * 64; i += NT)
    lds_st<float>(S_A + 4 * i, bf_lo(lds_ld<u16>(L_ANT + ((i >> 9) * 64 + (i & 63)) * 16 + ((i >> 6) & 7) * 2)));
  for (int i = t; i < 8 * 40 * ROW; i += NT) lds_st<float>(S_ACC + 4 * i, 0.f);
  __syncthreads();
  const float wx = lds_ld<float>(L_WX + 4 * lane);
  const u32x4 wv = lds_ld<u32x4>(L_WXF + 16 * lane);
  u32 sb[6];
  {
    const int pc = wave * 32 + (lane & 31);
    u32x4 g = __builtin_amdgcn_raw_buffer_load_b128(c.hand, pc < nwg ? WS_GRAN + (pc * NWG_MAX + bucket) * 16 : 0xfffffff0u, 0, 16);
    if ((g.y >> 4) != c.tagA || (g.w >> 7) != (c.tagB & 0x1ffffff)) g = u32x4{0, 0, 0, 0};
    unpack_bounds(g, sb);
    sb[1] = (((g.w >> 4) & 7) * NWG_MAX + pc) * 4096;
  }
  for (int p = 0; p < 32; ++p) {
    const int pb = __builtin_amdgcn_readlane(sb[1], p);
    const int a = __builtin_amdgcn_readlane(sb[0], p), b = __builtin_amdgcn_readlane(sb[5], p);
    for (int i0 = a; i0 < b; i0 += 64) {
      const int cnt = min(64, b - i0);
      u32x4 r;
      for (u32 spins = 0; spins < SPIN_MAX; ++spins) {
        asm volatile("" ::: "memory");
        r = __builtin_amdgcn_raw_buffer_load_b128(c.hand, WS_PAY + (pb + i0 + min(lane, cnt - 1)) * 16, 0, 16);
        if (__builtin_amdgcn_ballot_w64((((r.y ^ c.tagLo) & 0xffff0000u) | ((r.w ^ c.tagHi) & TAG_HI_MASK)) != 0) == 0) break;
      }
      const int sn = min((int)(r.z >> 16), c.N - 1), dl = min((int)((r.w >> 4) & 63), c.nn - 1);
      const f32x4 pd = lds_ld<f32x4>(L_PD + dl * 16);
      const float fx = pd.x - bf_lo(c.pos[sn * 3]), fy = pd.y - bf_lo(c.pos[sn * 3 + 1]), fz = pd.z - bf_lo(c.pos[sn * 3 + 2]);
      const u32 q0 = (r.y & 0xffffu) | cvt_pk(0.f, fx * fx + fy * fy + fz * fz), q1 = cvt_pk(fx, fy);
      const u32 q2 = (r.z & 0xffffu) | cvt_pk(0.f, fz), q3 = sn | (dl << 16);
#pragma unroll 4
      for (int k = 0; k < cnt; ++k) {
        const u32 u0 = __builtin_amdgcn_readlane(r.x, k), u1 = __builtin_amdgcn_readlane(q0, k), u2 = __builtin_amdgcn_readlane(q1, k);
        const u32 u3 = __builtin_amdgcn_readlane(q2, k), u4 = __builtin_amdgcn_readlane(q3, k);
        const u32 bv = (u16)__builtin_amdgcn_raw_buffer_load_b16(c.hand, 2 * lane, WS_B + (u4 & 0xffff) * 128, 16);
        const float y = lds_ld<float>(S_A + (u4 >> 16) * 256 + 4 * lane) + bf_lo(bv) + bf_lo(u0) * bf_lo(wv.x) + bf_hi(u0) * bf_hi(wv.x) +
                        bf_lo(u1) * bf_lo(wv.y) + bf_hi(u1) * (bf_hi(wv.y) + bf_lo(wv.w)) + bf_lo(u3) * bf_lo(wv.z);
        const float m = round_bf(y * __builtin_amdgcn_rcpf(1.0f + __builtin_amdgcn_exp2f(y)));
        float cs = m * wx;
#pragma unroll
        for (int o = 32; o > 0; o >>= 1) cs += __int_as_float(__builtin_amdgcn_ds_bpermute(4 * (lane ^ o), __float_as_int(cs)));
        const u32 ra = S_ACC + ((wave * 40 + (u4 >> 16)) * ROW) * 4;
        lds_st<float>(ra + 4 * lane, lds_ld<float>(ra + 4 * lane) + m);
        if (lane < 3) lds_st<float>(ra + 256 + 4 * lane, lds_ld<float>(ra + 256 + 4 * lane) + (lane == 0 ? bf_lo(u2) : lane == 1 ? bf_hi(u2) : bf_hi(u3)) * cs);
      }
    }
  }
  __syncthreads();
  for (int dl = wave; dl < c.nn; dl += 8) {
    float m = 0.f, ps = 0.f;
    for (int w = 0; w < 8; ++w) {
      m += lds_ld<float>(S_ACC + ((w * 40 + dl) * ROW + lane) * 4);
      ps += lds_ld<float>(S_ACC + ((w * 40 + dl) * ROW + 64 + (lane & 3)) * 4);
    }
    const int dn = c.d0 + dl;
    const u32 hv = c.h[dn * 64 + lane] | cvt_pk(0.f, m * NEG_LN2);
    float x = 0.f;
#pragma unroll 4
    for (int k = 0; k < 64; ++k) {
      const u32 v = __builtin_amdgcn_readlane(hv, k);
      x += bf_lo(v) * bf_lo(lds_ld<u16>(L_W + k * 128 + 2 * lane)) + bf_hi(v) * bf_lo(lds_ld<u16>(L_W + (64 + k) * 128 + 2 * lane));
    }
    c.out_h[dn * 64 + lane] = (u16)cvt_pk(bf_lo(hv) + silu_f(x), 0.f);
    if (lane < 3) c.out_pos[dn * 3 + lane] = (u16)cvt_pk(lds_ld<float>(L_PD + dl * 16 + 4 * lane) + round_bf(ps), 0.f);
  }
}

__global__ __launch_bounds__(NT, 2) void egnn_kernel(
    const u16* __restrict__ h, const u16* __restrict__ pos, const u32x2* __restrict__ eattr,
    const int* __restrict__ src, const int* __restrict__ dst, const u16* __restrict__ w_e,
    const u16* __restrict__ w_x, const u16* __restrict__ w_h, u16* __restrict__ out_h,
    u16* __restrict__ out_pos, char* __restrict__ ws, int N, int E, int chunk, int nwg, int base, int rem, int m1, int m2) {
  u32 cpre;
  {
    u64 pc;
    const u32 line = (blockIdx.x >> 3) * 8 + (threadIdx.x & 7);
    asm volatile("s_getpc_b64 %1\n\tv_cmpx_le_u32 vcc, 504, %3\n\ts_cbranch_execz .Lc%=\n\tglobal_load_dword %0, %2, %1\n\t.Lc%=:\n\ts_mov_b64 exec, -1\n\ts_nop 2"
                 : "=&v"(cpre), "=&s"(pc) : "v"(min(line, (u32)CODE_LINES) * 128), "v"(threadIdx.x) : "vcc", "memory");
    SCHED_FENCE();
  }
  asm volatile("" ::"s"(h), "s"(pos), "s"(eattr), "s"(src), "s"(dst), "s"(w_e), "s"(w_x), "s"(w_h), "s"(out_h),
               "s"(out_pos), "s"(ws), "s"(N), "s"(E), "s"(chunk), "s"(nwg), "s"(base), "s"(rem), "s"(m1), "s"(m2));
  const int t = threadIdx.x, lane = t & 63, tr = t & 255;
  const int wave = __builtin_amdgcn_readfirstlane(t >> 6);
  const int r32 = lane & 31, hh = lane >> 5;
  const int q4 = (lane >> 2) & 3, p4 = lane & 3, b4 = (lane >> 4) & 1;
  const int ebeg = blockIdx.x * chunk;
  const int n = max(0, min(E - ebeg, chunk));
  const __amdgpu_buffer_rsrc_t rd = __builtin_amdgcn_make_buffer_rsrc((void*)(dst + ebeg), 0, n * 4, 0x00020000);
  u64 epoch; u32x2 dv[5];
  asm volatile(
      "s_cmp_gt_u32 %[wv], 3\n\ts_cbranch_scc1 .Lp%=\n\t"
      "global_load_dwordx2 %[ep], %[z], %[ws] sc1\n\t"
      "buffer_load_dwordx2 %[d0], %[o8], %[rd], 0 offen nt\n\t"
      "buffer_load_dwordx2 %[d1], %[o8], %[rd], 0 offen offset:2048 nt\n\t"
      "buffer_load_dwordx2 %[d2], %[o8], %[rd], %[k4] offen nt\n\t"
      "buffer_load_dwordx2 %[d3], %[o8], %[rd], %[k4] offen offset:2048 nt\n\t"
      "buffer_load_dwordx2 %[d4], %[o8], %[rd], %[k8] offen nt\n\t"
      ".Lp%=:"
      : [ep] "=&v"(epoch), [d0] "=&v"(dv[0]), [d1] "=&v"(dv[1]), [d2] "=&v"(dv[2]), [d3] "=&v"(dv[3]), [d4] "=&v"(dv[4])
      : [z] "v"(WS_EPOCH), [ws] "s"(ws), [o8] "v"(8 * tr), [rd] "s"(rd), [k4] "s"(4096), [k8] "s"(8192), [wv] "s"(wave)
      : "memory", "scc");
  const int bucket = (blockIdx.x & 7) * (nwg >> 3) + min((int)(blockIdx.x & 7), nwg & 7) + (blockIdx.x >> 3);
  Ctx c;
  c.h = h; c.pos = pos; c.w_h = w_h; c.out_h = out_h; c.out_pos = out_pos;
  c.hand = __builtin_amdgcn_make_buffer_rsrc(ws, 0, (int)WS_HAND, 0x00020000);
  c.N = N; c.d0 = bucket * base + min(bucket, rem); c.nn = base + (bucket < rem ? 1 : 0); c.wave = wave;
  const __amdgpu_buffer_rsrc_t rp = __builtin_amdgcn_make_buffer_rsrc((void*)pos, 0, (N * 6 + 3) & ~3, 0x00020000);
  u32x4 wex; u32 small, warm;
  {
    const __amdgpu_buffer_rsrc_t rwe = __builtin_amdgcn_make_buffer_rsrc((void*)w_e, 0, 133 * 128, 0x00020000);
    const __amdgpu_buffer_rsrc_t rhr = __builtin_amdgcn_make_buffer_rsrc((void*)(h + c.d0 * 64), 0, c.nn * 128, 0x00020000);
    const __amdgpu_buffer_rsrc_t rsm = __builtin_amdgcn_make_buffer_rsrc((void*)(wave == 4 ? w_x : pos + c.d0 * 3 - 64), 0, 1024, 0x00020000);
    u32 keep;
    asm volatile(
        "s_cmp_lt_u32 %[wv], 4\n\ts_cbranch_scc1 .Lq%=\n\t"
        "s_mov_b32 %[keep], m0\n\ts_mov_b32 m0, %[dh]\n\ts_nop 0\n\t"
        "buffer_load_dwordx4 %[oh], %[rhr], %[sh] offen lds\n\t"
        "s_mov_b32 m0, %[dw]\n\ts_nop 0\n\t"
        "buffer_load_dwordx4 %[ol], %[rwe], %[sw] offen lds\n\t"
        "buffer_load_dwordx4 %[ol], %[rwe], %[sw] offen offset:1024 lds\n\t"
        "buffer_load_dwordx4 %[ol], %[rwe], %[sw] offen offset:2048 lds\n\t"
        "buffer_load_dwordx4 %[ol], %[rwe], %[sw] offen offset:3072 lds\n\t"
        "s_cmp_eq_u32 %[wv], 7\n\ts_cbranch_scc1 .Lq7%=\n\t"
        "buffer_load_ushort %[sm], %[o2], %[rsm], 0 offen\n\t"
        "s_branch .Lqe%=\n\t"
        ".Lq7%=:\n\ts_mov_b32 m0, %[dh2]\n\ts_nop 0\n\t"
        "buffer_load_dwordx4 %[oh], %[rhr], %[k4] offen lds\n\t"
        "buffer_load_dwordx4 %[wx], %[ol], %[rwe], %[k16] offen\n\t"
        "buffer_load_dword %[pw], %[v], %[rp], 0 offen\n\t"
        "global_load_dword %[sm], %[vh], %[wh]\n\t"
        ".Lqe%=:\n\ts_mov_b32 m0, %[keep]\n\t"
        ".Lq%=:"
        : [keep] "=&s"(keep), [pw] "=&v"(warm), [wx] "=&v"(wex), [sm] "=&v"(small)
        : [ol] "v"(16 * lane), [oh] "v"((lane >> 3) * 128 + ((lane ^ (lane >> 3)) & 7) * 16),
          [o2] "v"(tr < 64 + 3 * c.nn ? 2 * tr : 4096), [v] "v"(lane < 16 ? (blockIdx.x >> 3) * 2048 + lane * 128 : 0x10000000u),
          [vh] "v"((((blockIdx.x >> 3) & 31) * 4 + (lane & 3)) * 128), [wh] "s"(w_h), [rwe] "s"(rwe), [rhr] "s"(rhr), [rsm] "s"(rsm), [rp] "s"(rp), [wv] "s"(wave), [dh] "s"(L_HROW + (wave & 3) * 1024),
          [sh] "s"((wave & 3) * 1024), [dh2] "s"(L_HROW + 4096), [dw] "s"(L_W + (wave & 3) * 4096), [sw] "s"((wave & 3) * 4096),
          [k4] "s"(4096), [k16] "s"(16384)
        : "memory", "scc");
  }
  lds_st<u32x4>(L_HIST + 16 * t, u32x4{0, 0, 0, 0});
  lds_st<u32>(t < 80 ? L_OH + 4 * t : L_DUMP, (t >> 3) == (t & 3) && t < 32 ? (0x3f80u << (4 * (t & 4))) : 0u);
  if (wave == 0) lds_st<u32>(L_CLK, (u32)__builtin_amdgcn_s_memrealtime());
  __syncthreads();
  SCHED_FENCE();
  const u32 trOff = (u32)((32 * hh + q4) * 128 + (16 * b4 + 4 * p4) * 2);
  const u32 gbuf = L_POS + wave * 4096;
  u32x8 K, G, Q, ID, EP;
  u32x16 PS;
  u32x4 U;
  u32x3 F;
  auto loop_consts = [&]() {
    G[0] = G[1] = G[4] = G[5] = (u32)(b4 * 4 + (p4 ^ hh)) * 16;
    G[2] = G[3] = G[6] = G[7] = (u32)(b4 * 4 + (p4 ^ (2 | hh))) * 16;
    K[0] = L_R1 + (4 * hh + q4) * 16 + 512;
    K[1] = (u32)(r32 - 4 * hh - q4) * 16;
    K[2] = L_R2 + (4 * hh + q4) * 8 + ((u32)(4 * b4 + p4) << 20);
    K[3] = 0xffff0000u;
#pragma unroll
    for (int k = 0; k < 4; ++k)
      K[4 + k] = gbuf + (r32 >> 3) * 1024 + (32 * ((r32 >> 2) & 1) + 16 * (k >> 1) + 4 * (r32 & 3) + ((2 * (k & 1) + hh) ^ ((r32 >> 2) & 3))) * 16;
#pragma unroll
    for (int e = 0; e < 8; ++e) ID[e] = (r32 >> 1) == 8 * (e >> 2) + 4 * hh + (e & 3) ? 0x3f80u << (16 * (r32 & 1)) : 0u;
    U.x = U.y = U.z = (u32)(hh ? L_ANT : L_WXF) + r32 * 16;
    U.w = hh ? 1024u : 0u;
    {
      const int node = ((wave >> 1) & 1) * 32 + r32, row = min(node, 39);
#pragma unroll
      for (int k = 0; k < 4; ++k) EP[k] = L_R1 + row * 128 + (((2 * k + hh) ^ row) & 7) * 16;
      EP[4] = L_R1 + wave * 1024 + (lane >> 3) * 128 + ((lane ^ (lane >> 3)) & 7) * 16;
      EP[5] = node * 128 + (wave & 1) * 64 + 8 * hh;
      EP[6] = EP[7] = EP[5];
    }
    F = u32x3{(u32)(L_WX + 16 * hh), (u32)(L_SLAB + (r32 >> 2) * 256 + 16 * hh), (u32)(L_SLAB + 2048 + 4 * lane)};
    asm volatile("v_mov_b64 v[104:105], 0\n\tv_mov_b64 v[106:107], 0\n\tv_mov_b64 v[108:109], 0\n\tv_mov_b64 v[110:111], 0\n\tv_mov_b64 v[112:113], 0\n\tv_mov_b64 v[114:115], 0\n\tv_mov_b64 v[116:117], 0\n\tv_mov_b64 v[118:119], 0\n\tv_mov_b64 v[48:49], 0\n\tv_mov_b64 v[50:51], 0\n\tv_mov_b64 v[52:53], 0\n\tv_mov_b64 v[54:55], 0"
                 : "={v[104:119]}"(PS), "={v[48:55]}"(Q), "+{v[88:95]}"(K), "+{v[96:103]}"(G), "+{v[40:43]}"(U), "+{v[80:82]}"(F), "+{v[128:135]}"(ID), "+{v[136:143]}"(EP));
  };
  u32 epLo, epHi, padAddr = L_DUMP;
  u32 ent[SLOTS]; u32x4 rc[SLOTS];
  int total;
  auto read_list = [&]() {
    sy_wait(SY_LIST, 4);
    const int lbase = (wave < 4 ? 256 * SLOTS : 0) + tr;
    const u32 tv = lds_ld<u32>(L_PLAN + 24);
#pragma unroll
    for (int j = 0; j < SLOTS; ++j) ent[j] = lds_ld<u32>(L_IDX + 4 * (lbase + 256 * j));
    sy_arrive(SY_F);
#pragma unroll
    for (int j = 0; j < SLOTS; ++j) ent[j] = lbase + 256 * j < (int)tv ? ent[j] : 0x0f000000u;
    total = __builtin_amdgcn_readfirstlane(tv);
  };

  if (__builtin_expect(wave < 4, 1)) {
    __builtin_amdgcn_s_setprio(2);
    const __amdgpu_buffer_rsrc_t rs = __builtin_amdgcn_make_buffer_rsrc((void*)(src + ebeg), 0, n * 4, 0x00020000);
    const __amdgpu_buffer_rsrc_t ra = __builtin_amdgcn_make_buffer_rsrc((void*)(eattr + ebeg), 0, n * 8, 0x00020000);
    SCHED_FENCE();
    u32x2 sv[5]; u32x4 av[5];
#pragma unroll
    for (int i = 0; i < 5; ++i) {
      av[i] = __builtin_amdgcn_raw_buffer_load_b128(ra, 16 * tr, 4096 * i, 2);
      sv[i] = __builtin_amdgcn_raw_buffer_load_b64(rs, 8 * tr, 2048 * i, 2);
    }
    SCHED_FENCE();
    const u32 slot = (__builtin_amdgcn_readfirstlane(lds_ld<u32>(L_CLK)) >> 11) & 7;
    asm volatile("s_waitcnt vmcnt(10)" : "+v"(epoch), "+v"(dv[0]), "+v"(dv[1]), "+v"(dv[2]), "+v"(dv[3]), "+v"(dv[4])::"memory");
    u32 ka[10], rk[10], meta[10];
#pragma unroll
    for (int j = 0; j < 10; ++j) {
      const int d = dv[j >> 1][j & 1], q = max(__mul24(d, m1), __mul24(d - rem, m2)) >> 20;
      const u32 dl = d - min(q, rem) - __mul24(q, base);
      const bool ok = (j >> 1) * 512 + 2 * tr + (j & 1) < n;
      ka[j] = ok ? 32 * q + ((dl >> 1) & ~3u) : 28;
      meta[j] = dl << 4;
      rk[j] = __hip_atomic_fetch_add((LAS u32*)(uintptr_t)(L_HIST + ka[j]), 1u, __ATOMIC_RELAXED, __HIP_MEMORY_SCOPE_WORKGROUP);
    }
    sy_arrive(SY_HIST);
    epLo = __builtin_amdgcn_readfirstlane((u32)epoch); epHi = __builtin_amdgcn_readfirstlane((u32)(epoch >> 32));
    make_tags(c, epLo, epHi);
    if (wave == 0) { lds_st<u32x2>(L_SYNC + 32, u32x2{epLo, epHi}); sy_arrive(SY_EPOCH); }
    sy_wait(SY_HIST, 4);
    const u32 skew = (blockIdx.x * 5 % 12) * 128;
    {
      u32x4 c4[4]; u32 c5[4];
#pragma unroll
      for (int k = 0; k < 4; ++k) {
        const u32 a = L_HIST + 32 * (((wave - k) & 3) * 64 + lane);
        c4[k] = lds_ld<u32x4>(a); c5[k] = lds_ld<u32>(a + 16);
      }
      u32 x = 0;
#pragma unroll
      for (int k = 1; k < 4; ++k) x += k <= wave ? c4[k].x + c4[k].y + c4[k].z + c4[k].w + c5[k] : 0u;
      x = scan64((x << 16) + c4[0].x + c4[0].y + c4[0].z + c4[0].w + c5[0]);
      const u32 e5 = (x & 0xffff) + (__builtin_amdgcn_readlane(x, 63) >> 16) + skew;
      const u32 b4_ = e5 - c5[0], b3 = b4_ - c4[0].w, b2 = b3 - c4[0].z, b1 = b2 - c4[0].y, b0 = b1 - c4[0].x;
      lds_st<u32x4>(L_START + 32 * tr, u32x4{b0, b1, b2, b3});
      lds_st<u32>(L_START + 32 * tr + 16, b4_);
      sy_arrive(SY_START);
      const u32x4 g = {b0 | (b1 << 12) | (b2 << 24), (b2 >> 8) | (c.tagA << 4), b3 | (b4_ << 12) | (e5 << 24), (e5 >> 8) | (slot << 4) | (c.tagB << 7)};
      __builtin_amdgcn_raw_buffer_store_b128(g, c.hand, 16 * tr, WS_GRAN + blockIdx.x * (NWG_MAX * 16), 16);
      STORE_PAD();
    }
    sy_wait(SY_START, 4);
    {
      u32 st[10];
#pragma unroll
      for (int j = 0; j < 10; ++j) st[j] = lds_ld<u32>(L_START + ka[j]);
#pragma unroll
      for (int j = 0; j < 10; ++j) st[j] = ka[j] != 28 ? L_STG - skew * 16 + (st[j] + rk[j]) * 16 : L_DUMP;
#pragma unroll
      for (int j = 0; j < 10; ++j) {
        const u32 e0 = av[j >> 1][2 * (j & 1)], e1 = av[j >> 1][2 * (j & 1) + 1];
        lds_st<u32x4>(st[j], u32x4{e0, (e1 & 0xffffu) | c.tagLo, (e1 >> 16) | (sv[j >> 1][j & 1] << 16), meta[j] | c.tagHi});
      }
    }
    sy_arrive(SY_STG);
    sy_wait(SY_STG, 4);
    {
      const __amdgpu_buffer_rsrc_t ro = __builtin_amdgcn_make_buffer_rsrc(ws + WS_PAY + ((slot * NWG_MAX + blockIdx.x) * 4096 + skew) * 16, 0, n * 16, 0x00020000);
      u32x4 x[10];
#pragma unroll
      for (int j = 0; j < 10; ++j) x[j] = lds_ld<u32x4>(L_STG + 16 * tr + 4096 * j);
#pragma unroll
      for (int j = 0; j < UNGATED; ++j) __builtin_amdgcn_raw_buffer_store_b128(x[j], ro, 16 * tr, j * 4096, 16);
      STORE_PAD();
      loop_consts();
      sy_wait(SY_B, 2);
      __builtin_amdgcn_raw_buffer_store_b64(u32x2{c.tagA, c.tagB}, c.hand, t == 0 ? WS_FLAG + blockIdx.x * 8 : 0xfffffff0u, 0, 16);
      sy_wait(SY_BND, 4);
#pragma unroll
      for (int j = UNGATED; j < 10; ++j) __builtin_amdgcn_raw_buffer_store_b128(x[j], ro, 16 * tr, j * 4096, 16);
      STORE_PAD();
      {
        const u32 pb = min(wave * 15, 44) * 1024;
        u32 keep, so;
        asm volatile(
            "s_mov_b32 %[keep], m0\n\ts_mov_b32 m0, %[d]\n\ts_mov_b32 %[so], 0\n\t"
            ".Lpos%=:\n\t"
            "buffer_load_dwordx4 %[v], %[rp], %[so] offen offset:0 lds\n\t"
            "buffer_load_dwordx4 %[v], %[rp], %[so] offen offset:1024 lds\n\t"
            "buffer_load_dwordx4 %[v], %[rp], %[so] offen offset:2048 lds\n\t"
            "s_add_u32 m0, m0, 0xc00\n\ts_add_u32 %[so], %[so], 0xc00\n\t"
            "s_cmp_lt_u32 %[so], 0x3c00\n\ts_cbranch_scc1 .Lpos%=\n\t"
            "s_mov_b32 m0, %[keep]"
            : [keep] "=&s"(keep), [so] "=&s"(so)
            : [v] "v"(pb + lane * 16), [rp] "s"(rp), [d] "s"(L_POS + pb)
            : "memory", "scc");
      }
    }
    SCHED_FENCE();
    asm volatile("s_waitcnt vmcnt(0)" ::: "memory");
    read_list();
  } else {
    loop_consts();
    u32 smallAddr;
    {
      const int x3 = tr - 64, nd3 = x3 / 3;
      smallAddr = tr < 64 ? L_WX + 4 * tr : x3 < 120 ? L_PD + 4 * (x3 + nd3) : L_DUMP;
    }
    asm volatile("s_waitcnt vmcnt(0)" : "+v"(cpre), "+v"(warm), "+v"(wex), "+v"(small), "+v"(smallAddr)
                 :: "memory");
    sy_arrive(SY_W);
    lds_st<float>(smallAddr, bf_lo(small) * (tr < 64 ? NEG_LN2 : 1.0f));
    if (wave == 7) {
      const u32 row = lane >> 3, ks = row == 0 ? 3 : row < 4 ? row - 1 : row;
      const u32 a = row == 6 ? 0x10000000u : L_WXF + (lane & 7) * 128 + ks * 2;
      const u32 a2 = row == 0 ? L_WXF + (lane & 7) * 128 + 12 : 0x10000000u;
#pragma unroll
      for (int e = 0; e < 4; ++e) {
        const float x0 = bf_lo(wex[e]) * NEG_LOG2E, x1 = bf_hi(wex[e]) * NEG_LOG2E;
        const u32 y = cvt_pk(x0, x1), z = cvt_pk(x0 - bf_lo(y), x1 - bf_hi(y));
        lds_st<u16>(a + 32 * e, (u16)y); lds_st<u16>(a + 32 * e + 16, (u16)(y >> 16));
        lds_st<u16>(a2 + 32 * e, (u16)z); lds_st<u16>(a2 + 32 * e + 16, (u16)(z >> 16));
      }
    }
    sy_wait(SY_W, 4);
    u32x4 brow[4] = {};
    {
      auto hfrag = [&](int rt, int k) {
        const int row = min(rt * 32 + r32, 39);
        return lds_ld<u32x4>(L_HROW + row * 128 + (((4 * hh + k) ^ row) & 7) * 16);
      };
      auto scaled = [&](const f32x16& a, int g) {
        return u32x2{cvt_pk(a[4 * g] * NEG_LOG2E, a[4 * g + 1] * NEG_LOG2E), cvt_pk(a[4 * g + 2] * NEG_LOG2E, a[4 * g + 3] * NEG_LOG2E)};
      };
      f32x16 a0, a1;
#pragma unroll
      for (int i = 0; i < 16; ++i) { a0[i] = 0.f; a1[i] = 0.f; }
      if (wave < 6) {
#pragma unroll
        for (int k = 0; k < 4; ++k) {
          const u32x4 hf = hfrag(wave & 1, k);
          a0 = MFMA(lds_tr2(L_W + trOff + (64 + 8 * k) * 128), hf, a0);
          a1 = MFMA(lds_tr2(L_W + trOff + (64 + 8 * k) * 128 + 64), hf, a1);
        }
        const u32 tb = L_IDX + (wave & 1) * 4608;
#pragma unroll
        for (int g = 0; g < 4; ++g) {
          lds_st<u32x2>(tb + r32 * 144 + 16 * g + 8 * hh, scaled(a0, g));
          lds_st<u32x2>(tb + r32 * 144 + 64 + 16 * g + 8 * hh, scaled(a1, g));
        }
#pragma unroll
        for (int i = 0; i < 4; ++i) brow[i] = lds_ld<u32x4>(tb + (8 * i + (lane >> 3)) * 144 + (lane & 7) * 16);
      } else {
        const int nt = wave & 1;
#pragma unroll
        for (int k = 0; k < 4; ++k) {
          const u32x4 wf = lds_tr2(L_W + trOff + nt * 64 + 8 * k * 128);
          a0 = MFMA(hfrag(0, k), wf, a0);
          a1 = MFMA(hfrag(1, k), wf, a1);
        }
        const u32 a = L_ANT + (nt * 32 + r32) * 16 + 8 * hh;
#pragma unroll
        for (int g = 0; g < 4; ++g) lds_st<u32x2>(a + g * 1024, scaled(a0, g));
        lds_st<u32x2>(a + 4096, scaled(a1, 0));
      }
    }
    {
      const __amdgpu_buffer_rsrc_t rb = __builtin_amdgcn_make_buffer_rsrc(ws + WS_B + c.d0 * 128, 0, c.nn * 128, 0x00020000);
      if (wave < 6) {
#pragma unroll
        for (int i = 0; i < 4; ++i) __builtin_amdgcn_raw_buffer_store_b128(brow[i], rb, 16 * lane + 1024 * i, (wave & 1) * 4096, 16);
        STORE_PAD();
      }
    }
    sy_wait(SY_EPOCH, 1);
    {
      const u32x2 e = lds_ld<u32x2>(L_SYNC + 32);
      epLo = __builtin_amdgcn_readfirstlane(e.x); epHi = __builtin_amdgcn_readfirstlane(e.y);
      make_tags(c, epLo, epHi);
    }
    sy_wait(SY_STG, 1);
    u32x4 gv = {0, 0, 0, 0};
    {
      const u32 goff = WS_GRAN + (tr * NWG_MAX + bucket) * 16;
      bool bad = tr < nwg;
#pragma clang loop unroll(disable)
      for (u32 spins = 0; spins < SPIN_MAX; ++spins) {
        asm volatile("" ::: "memory");
        if (bad) gv = __builtin_amdgcn_raw_buffer_load_b128(c.hand, goff, 0, 16);
        bad = bad && ((gv.y >> 4) != c.tagA || (gv.w >> 7) != (c.tagB & 0x1ffffff));
        if (spins == 0 && wave < 6) { asm volatile("s_waitcnt vmcnt(0)" ::: "memory"); sy_arrive(SY_B); }
        if (__builtin_amdgcn_ballot_w64(bad) == 0) break;
      }
      if (bad) gv = u32x4{0, 0, 0, 0};
    }
    sy_arrive(SY_BND);
    u32 s[6];
    unpack_bounds(gv, s);
    const u32 len = s[5] - s[0];
    const u32 cA = (s[1] - s[0]) | ((s[2] - s[1]) << 16), cB = (s[3] - s[2]) | ((s[4] - s[3]) << 16);
    const u32 iA = scan64(cA), iB = scan64(cB), iT = scan64(len);
    lds_st<u32x4>(lane == 63 ? L_TOT + 16 * (wave - 4) : L_DUMP, u32x4{iA, iB, iT, iT});
    sy_arrive(SY_TOT);
    sy_wait(SY_TOT, 4);
    u32 tw[NWIN + 1], cw[NWIN], mA, mB, mT;
    {
      const u32x4 v = lds_ld<u32x4>(L_TOT + 16 * (lane & 3));
      u32x4 x = v;
#pragma unroll
      for (int k = 0; k < 3; ++k) {
        x[k] += __builtin_amdgcn_update_dpp(0u, x[k], 0x111, 0xf, 0xf, true);
        x[k] += __builtin_amdgcn_update_dpp(0u, x[k], 0x112, 0xf, 0xf, true);
      }
      const u32 aA = __builtin_amdgcn_readlane(x.x, 3), aB = __builtin_amdgcn_readlane(x.y, 3);
      total = __builtin_amdgcn_readlane(x.z, 3);
      mA = __builtin_amdgcn_readlane(x.x - v.x, wave - 4); mB = __builtin_amdgcn_readlane(x.y - v.y, wave - 4);
      mT = __builtin_amdgcn_readlane(x.z - v.z, wave - 4);
      cw[0] = aA & 0xffff; cw[1] = aA >> 16; cw[2] = aB & 0xffff; cw[3] = aB >> 16;
      cw[4] = total - cw[0] - cw[1] - cw[2] - cw[3];
      tw[0] = 0;
#pragma unroll
      for (int w = 0; w < NWIN; ++w) tw[w + 1] = tw[w] + ((cw[w] + 31) >> 5);
    }
    const bool fits = (u32)total <= (u32)CAP && tw[NWIN] <= (u32)CAPT;
    const u32 pre = iT - len + mT;
    {
      u32 a = L_IDX + 4 * pre, v = ((gv.w & 0x70) << 16) | ((u32)tr << 12) | s[0];
      int rem = fits ? (int)len : 0;
      asm volatile(
          ".Lex%=:\n\t"
          "v_cmpx_lt_i32 vcc, 0, %[rem]\n\tds_write_b32 %[a], %[v] offset:0\n\tv_add_u32 %[v], 1, %[v]\n\t"
          "v_cmpx_lt_i32 vcc, 1, %[rem]\n\tds_write_b32 %[a], %[v] offset:4\n\tv_add_u32 %[v], 1, %[v]\n\t"
          "v_cmpx_lt_i32 vcc, 2, %[rem]\n\tds_write_b32 %[a], %[v] offset:8\n\tv_add_u32 %[v], 1, %[v]\n\t"
          "v_cmpx_lt_i32 vcc, 3, %[rem]\n\tds_write_b32 %[a], %[v] offset:12\n\tv_add_u32 %[v], 1, %[v]\n\t"
          "v_cmpx_lt_i32 vcc, 4, %[rem]\n\tds_write_b32 %[a], %[v] offset:16\n\tv_add_u32 %[v], 1, %[v]\n\t"
          "v_cmpx_lt_i32 vcc, 5, %[rem]\n\tds_write_b32 %[a], %[v] offset:20\n\tv_add_u32 %[v], 1, %[v]\n\t"
          "v_cmpx_lt_i32 vcc, 6, %[rem]\n\tds_write_b32 %[a], %[v] offset:24\n\tv_add_u32 %[v], 1, %[v]\n\t"
          "v_cmpx_lt_i32 vcc, 7, %[rem]\n\tds_write_b32 %[a], %[v] offset:28\n\tv_add_u32 %[v], 1, %[v]\n\t"
          "v_cmpx_lt_i32 vcc, 8, %[rem]\n\tds_write_b32 %[a], %[v] offset:32\n\tv_add_u32 %[v], 1, %[v]\n\t"
          "v_cmpx_lt_i32 vcc, 9, %[rem]\n\tds_write_b32 %[a], %[v] offset:36\n\tv_add_u32 %[v], 1, %[v]\n\t"
          "v_cmpx_lt_i32 vcc, 10, %[rem]\n\tds_write_b32 %[a], %[v] offset:40\n\tv_add_u32 %[v], 1, %[v]\n\t"
          "v_cmpx_lt_i32 vcc, 11, %[rem]\n\tds_write_b32 %[a], %[v] offset:44\n\tv_add_u32 %[v], 1, %[v]\n\t"
          "v_cmpx_lt_i32 vcc, 12, %[rem]\n\tds_write_b32 %[a], %[v] offset:48\n\tv_add_u32 %[v], 1, %[v]\n\t"
          "v_cmpx_lt_i32 vcc, 13, %[rem]\n\tds_write_b32 %[a], %[v] offset:52\n\tv_add_u32 %[v], 1, %[v]\n\t"
          "v_cmpx_lt_i32 vcc, 14, %[rem]\n\tds_write_b32 %[a], %[v] offset:56\n\tv_add_u32 %[v], 1, %[v]\n\t"
          "v_cmpx_lt_i32 vcc, 15, %[rem]\n\tds_write_b32 %[a], %[v] offset:60\n\tv_add_u32 %[v], 1, %[v]\n\t"
          "v_cmpx_lt_i32 vcc, 16, %[rem]\n\tds_write_b32 %[a], %[v] offset:64\n\tv_add_u32 %[v], 1, %[v]\n\t"
          "v_cmpx_lt_i32 vcc, 17, %[rem]\n\tds_write_b32 %[a], %[v] offset:68\n\tv_add_u32 %[v], 1, %[v]\n\t"
          "v_cmpx_lt_i32 vcc, 18, %[rem]\n\tds_write_b32 %[a], %[v] offset:72\n\tv_add_u32 %[v], 1, %[v]\n\t"
          "v_cmpx_lt_i32 vcc, 19, %[rem]\n\tds_write_b32 %[a], %[v] offset:76\n\tv_add_u32 %[v], 1, %[v]\n\t"
          "v_cmpx_lt_i32 vcc, 20, %[rem]\n\tds_write_b32 %[a], %[v] offset:80\n\tv_add_u32 %[v], 1, %[v]\n\t"
          "v_cmpx_lt_i32 vcc, 21, %[rem]\n\tv_subrev_u32 %[rem], 21, %[rem]\n\tv_add_u32 %[a], 0x54, %[a]\n\t"
          "s_cbranch_execnz .Lex%=\n\ts_mov_b64 exec, -1\n\ts_nop 1"
          : [a] "+v"(a), [v] "+v"(v), [rem] "+v"(rem)::"vcc", "memory");
    }
    lds_st<u32>(L_PLAN + 24, fits ? total : 0u);
    sy_arrive(SY_LIST);
    if (wave == 7) {
      lds_st<u32x4>(L_PLAN, u32x4{0, tw[1], tw[2], tw[3]});
      lds_st<u32x2>(L_PLAN + 16, u32x2{tw[4], tw[5]});
      lds_st<u32>(L_PLAN + 28, fits ? 0u : 1u);
      const int ntiles = tw[NWIN], k = (lane & 8 ? lane >> 1 : lane) & 3;
      const int a = (ntiles * k) >> 2, b = (ntiles * (k + 1)) >> 2;
      const int lo = a + (((b - a) * CUT_MIN + 63) >> 6), hi = b - (lo - a);
      int m = a + (((b - a) * HI_SHARE + 32) >> 6);
#pragma unroll
      for (int w = 1; w < NWIN; ++w) m = (int)tw[w] >= lo && (int)tw[w] <= hi ? (int)tw[w] : m;
      const bool second = lane & 8 ? lane & 1 : (b - m > m - a) == ((lane & 4) == 0);
      const int order = 2 * k + (second ? 1 : 0), t0 = second ? m : a, t1 = second ? b : m;
      int w0 = 0;
#pragma unroll
      for (int w = 1; w < NWIN; ++w) w0 += (int)tw[w] <= t0 ? 1 : 0;
      const int tprio = (lane & 4) ? t0 + (((t1 - t0) * PRIO_A - (b - a) * PRIO_B) >> 6) : -1;
      lds_st<u32x4>(lane < 8 ? L_PLAN + 64 + 16 * lane : L_DUMP, u32x4{(u32)t0, (u32)t1, (u32)(order | (w0 << 8)), (u32)tprio});
      u32 sm[NWIN];
#pragma unroll
      for (int w = 0; w < NWIN; ++w)
        sm[w] = ((u32)__builtin_amdgcn_ballot_w64(max(t0, (int)tw[w]) < min(t1, (int)tw[w + 1])) >> 8) & 0xff;
      lds_st<u32x4>(L_PLAN + 32, u32x4{sm[0], sm[1], sm[2], sm[3]});
      lds_st<u32>(L_PLAN + 48, sm[4]);
    } else {
      u32 cc = 0, ts = 0;
#pragma unroll
      for (int x = 0; x < NWIN; ++x) { cc = (tr >> 5) == x ? cw[x] : cc; ts = (tr >> 5) == x ? tw[x] : ts; }
      const u32 ps = cc + r32;
      padAddr = fits && ps < ((cc + 31) & ~31u) ? L_R1 + (ts * 32 + ps) * 16 : L_DUMP;
    }
    {
      const u32 xA = iA - cA + mA, xB = iB - cB + mB;
      const u32 x4 = pre - (xA & 0xffff) - (xA >> 16) - (xB & 0xffff) - (xB >> 16);
      const u32 a0 = (xA & 0xffff) - s[0], a1 = tw[1] * 32 + (xA >> 16) - s[1];
      const u32 a2 = tw[2] * 32 + (xB & 0xffff) - s[2], a3 = tw[3] * 32 + (xB >> 16) - s[3];
      const u32 a4 = tw[4] * 32 + x4 - s[4];
      lds_st<u32x4>(L_ADJ + 16 * tr, u32x4{(a0 & 0xffff) | (a1 << 16), (a2 & 0xffff) | (a3 << 16), a4, a4});
    }
    read_list();
    if (blockIdx.x == 0 && tr == 0)
      __hip_atomic_store((GAS u64*)(ws + WS_EPOCH), (((u64)epHi << 32) | epLo) + 1, __ATOMIC_RELAXED, __HIP_MEMORY_SCOPE_AGENT);
    {
      const __amdgpu_buffer_rsrc_t rw = __builtin_amdgcn_make_buffer_rsrc((void*)w_h, 0, 128 * 128, 0x00020000);
      u32 keep;
      asm volatile(
          "s_mov_b32 %[keep], m0\n\ts_mov_b32 m0, %[d]\n\ts_nop 0\n\t"
          "buffer_load_dwordx4 %[v], %[rs], 0 offen lds\n\t"
          "buffer_load_dwordx4 %[v], %[rs], 0 offen offset:1024 lds\n\t"
          "buffer_load_dwordx4 %[v], %[rs], 0 offen offset:2048 lds\n\t"
          "buffer_load_dwordx4 %[v], %[rs], 0 offen offset:3072 lds\n\t"
          "s_mov_b32 m0, %[keep]"
          : [keep] "=&s"(keep)
          : [v] "v"((wave - 4) * 4096 + lane * 16), [rs] "s"(rw), [d] "s"(L_W + (wave - 4) * 4096)
          : "memory");
    }
  }

  u32 mlo[SLOTS], mhi[SLOTS], arow[SLOTS]; float sp[SLOTS][3];
#pragma unroll
  for (int j = 0; j < SLOTS; ++j) {
    mlo[j] = ent[j] < 0x0f000000u ? 0xffff0000u : 0u; mhi[j] = ent[j] < 0x0f000000u ? TAG_HI_MASK : 0u;
    arow[j] = L_ADJ + ((ent[j] >> 8) & 0xff0);
    asm volatile("v_mov_b32 %3, 0\n\tv_mov_b32 %4, 0\n\tv_mov_b32 %5, 0"
                 : "+v"(mlo[j]), "+v"(mhi[j]), "+v"(arow[j]), "=v"(sp[j][0]), "=v"(sp[j][1]), "=v"(sp[j][2]));
  }
#pragma unroll
  for (int j = 0; j < SLOTS; ++j) rc[j] = __builtin_amdgcn_raw_buffer_load_b128(c.hand, ent[j] << 4, WS_PAY, 16);
  SCHED_FENCE();
  sy_wait(SY_F, 8);
  U.x = lds_ld<u32>(L_PLAN + 4 * (lane & 15));
  const u32x4 plan = lds_ld<u32x4>(L_PLAN + 64 + 16 * wave);
  const int t0 = __builtin_amdgcn_readfirstlane(plan.x), t1 = __builtin_amdgcn_readfirstlane(plan.y);
  const int order = __builtin_amdgcn_readfirstlane(plan.z) & 0xff, tprio = __builtin_amdgcn_readfirstlane(plan.w);
  int w = __builtin_amdgcn_readfirstlane(plan.z) >> 8;
  if (__builtin_expect(__builtin_amdgcn_readlane(U.x, 7) != 0, 0)) {
    asm volatile("s_waitcnt vmcnt(0)" ::: "memory");
    slow_bucket(c, bucket, nwg);
  } else {
  lds_st<u32x4>(padAddr, u32x4{0, 0, 0, 0x800000});
  const u32 foff = wave < 4 && tr < nwg ? WS_FLAG + 8 * tr : 0xfffffff0u;
  u32x2 fl;
  const int sbase = (wave < 4 ? 256 * SLOTS : 0) + (wave & 3) * 64;
#pragma unroll
  for (int jb = 0; jb < SLOTS; jb += 2) {
    if (jb < 4 || total > sbase + 256 * jb) {
      auto stale = [&](int j) { return ((rc[j].y ^ c.tagLo) & mlo[j]) | ((rc[j].w ^ c.tagHi) & mhi[j]); };
      if (__builtin_expect(__builtin_amdgcn_ballot_w64((stale(jb) | stale(jb + 1)) != 0) != 0, 0)) {
        u32 spins = 0;
        do {
          asm volatile("" ::: "memory");
#pragma unroll
          for (int j = jb; j < jb + 2; ++j)
            if (stale(j)) rc[j] = __builtin_amdgcn_raw_buffer_load_b128(c.hand, ent[j] << 4, WS_PAY, 16);
          asm volatile("" : "+v"(rc[jb]), "+v"(rc[jb + 1]));
        } while (__builtin_amdgcn_ballot_w64((stale(jb) | stale(jb + 1)) != 0) != 0 && ++spins < SPIN_MAX);
      }
      if (jb == 2) fl = __builtin_amdgcn_raw_buffer_load_b64(c.hand, foff, 0, 16);
      u32 adj[2]; f32x4 pd[2];
      asm volatile(
          "ds_read_u16_d16_hi %[x0], %[s0]\n\tds_read_u16_d16_hi %[y0], %[s0] offset:2\n\tds_read_u16_d16_hi %[z0], %[s0] offset:4\n\t"
          "ds_read_b128 %[p0], %[d0] offset:%[pd]\n\tds_read_u16 %[a0], %[r0]\n\t"
          "ds_read_u16_d16_hi %[x1], %[s1]\n\tds_read_u16_d16_hi %[y1], %[s1] offset:2\n\tds_read_u16_d16_hi %[z1], %[s1] offset:4\n\t"
          "ds_read_b128 %[p1], %[d1] offset:%[pd]\n\tds_read_u16 %[a1], %[r1]\n\t"
          "s_waitcnt lgkmcnt(0)"
          : [x0] "+v"(sp[jb][0]), [y0] "+v"(sp[jb][1]), [z0] "+v"(sp[jb][2]), [p0] "=&v"(pd[0]), [a0] "=&v"(adj[0]),
            [x1] "+v"(sp[jb + 1][0]), [y1] "+v"(sp[jb + 1][1]), [z1] "+v"(sp[jb + 1][2]), [p1] "=&v"(pd[1]), [a1] "=&v"(adj[1])
          : [s0] "v"(__umul24(rc[jb].z >> 16, 6u) + L_POS), [d0] "v"(rc[jb].w & 0x3f0), [r0] "v"(arow[jb] + ((rc[jb].w >> 7) & 7) * 2),
            [s1] "v"(__umul24(rc[jb + 1].z >> 16, 6u) + L_POS), [d1] "v"(rc[jb + 1].w & 0x3f0),
            [r1] "v"(arow[jb + 1] + ((rc[jb + 1].w >> 7) & 7) * 2), [pd] "i"(L_PD)
          : "memory");
#pragma unroll
      for (int k = 0; k < 2; ++k) {
        const int j = jb + k;
        const float fx = pd[k].x - sp[j][0], fy = pd[k].y - sp[j][1], fz = pd[k].z - sp[j][2];
        const float dd = fx * fx + fy * fy + fz * fz;
        const u32 d2 = cvt_pk(dd, dd);
        const u32 ps = (adj[k] + ent[j]) & 0x01000fffu;
        lds_st<u32x4>(L_R1 + ps * 16, u32x4{rc[j].x, (rc[j].y & 0xffffu) | (d2 & 0xffff0000u), rc[j].z, ((rc[j].w & 0x70u) << 16) | (d2 & 0xffffu)});
        lds_st<u32x2>(L_R2 + ps * 8, u32x2{cvt_pk(1.0f, fx), cvt_pk(fy, fz)});
      }
    }
  }
  if (__builtin_expect(__builtin_amdgcn_ballot_w64((int)foff >= 0 && (fl.x != c.tagA || fl.y != c.tagB)) != 0, 0)) {
    u32 spins = 0;
    do {
      asm volatile("" ::: "memory");
      fl = __builtin_amdgcn_raw_buffer_load_b64(c.hand, foff, 0, 16);
    } while (__builtin_amdgcn_ballot_w64((int)foff >= 0 && (fl.x != c.tagA || fl.y != c.tagB)) != 0 && ++spins < SPIN_MAX);
  }
  u32 hwarm;
  asm volatile("buffer_load_dword %0, %1, %2, 0 offen sc1" : "=v"(hwarm)
               : "v"(t < 320 ? WS_B + ((blockIdx.x >> 3) * 320 + t) * 128 : 0xfffffff0u), "s"(c.hand) : "memory");
  if (wave < 4) __builtin_amdgcn_s_setprio(1);
  else if (tprio > t0) __builtin_amdgcn_s_setprio(2);
  u32x4 hrs;
  {
    const u64 hb = (u64)(uintptr_t)ws + WS_B - 3072;
    hrs = u32x4{(u32)hb, ((u32)(hb >> 32) & 0xffffu) | (128u << 16), (u32)N, 0x00020000u};
  }
  {
    u32 sm = __builtin_amdgcn_readlane(U.x, 8 + min(wave, NWIN - 1)), sb3[3];
#pragma unroll
    for (int i = 0; i < 3; ++i) {
      sb3[i] = (sm ? L_SLAB + (wave + __builtin_ctz(sm)) * SLAB_BYTES : 0x10000000u) + lane * 32;
      sm &= sm - 1;
    }
    EP[6] = sb3[0]; EP[7] = sb3[1]; U.y = sb3[2];
  }
  u32 aD = K[0] - 512 + t0 * 512, aR = aD + K[1], aB = U.z + U.w * w;
  asm volatile("s_waitcnt vmcnt(1)" : "+v"(aD), "+v"(aR), "+v"(aB) : "v"(hwarm), "s"(hrs) : "memory");
  __syncthreads();

  f32x16 acc; u32x16 wm; u32x8 hres;
  auto mlp_prep = [&]() {
    const u32 mt = wave & 1;
    const u32 tro = L_W + (8 * hh + q4) * 128 + (16 * b4 + 4 * p4) * 2 + mt * 64;
    const int row = min(((wave >> 1) & 1) * 32 + r32, 39);
    const u32 hr = L_HROW + row * 128;
#pragma unroll
    for (int i = 0; i < 16; ++i) acc[i] = 0.f;
#pragma unroll
    for (int k = 0; k < 4; ++k)
      acc = MFMA(lds_tr2(tro + 16 * k * 128), lds_ld<u32x4>(hr + (((2 * k + hh) ^ row) & 7) * 16), acc);
#pragma unroll
    for (int k = 0; k < 4; ++k) {
      const u32x4 x = lds_tr2(tro + (64 + 16 * k) * 128);
      const u32x2 y = lds_ld<u32x2>(hr + (((4 * mt + k) ^ row) & 7) * 16 + 8 * hh);
      wm[4 * k] = x.x; wm[4 * k + 1] = x.y; wm[4 * k + 2] = x.z; wm[4 * k + 3] = x.w;
      hres[2 * k] = y.x; hres[2 * k + 1] = y.y;
    }
    asm volatile("" : "+{v[64:79]}"(acc), "+{v[24:39]}"(wm), "+{v[56:63]}"(hres));
  };
  {
    const u32 o1 = 1024, o2 = 2048, o3 = 3072;
    auto gather = [&](int tile, u32 buf) {
#pragma unroll
      for (int i = 0; i < 4; ++i) G[2 * i] = lds_ld<u16>(aD + tile * 512 + 128 * i + 10);
      u32 keep;
      asm volatile(
          "s_mov_b32 %[keep], m0\n\ts_mov_b32 m0, %[gbuf]\n\ts_nop 0\n\t"
          "buffer_load_dwordx4 v[96:97], %[rs], %[o3] idxen offen sc1 lds\n\t"
          "buffer_load_dwordx4 v[98:99], %[rs], %[o2] idxen offen offset:1024 sc1 lds\n\t"
          "buffer_load_dwordx4 v[100:101], %[rs], %[o1] idxen offen offset:2048 sc1 lds\n\t"
          "buffer_load_dwordx4 v[102:103], %[rs], 0 idxen offen offset:3072 sc1 lds\n\t"
          "s_mov_b32 m0, %[keep]"
          : [keep] "=&s"(keep)
          : "{v[96:103]}"(G), [rs] "s"(hrs), [gbuf] "s"(buf), [o1] "s"(o1), [o2] "s"(o2), [o3] "s"(o3)
          : "memory");
    };
    u32x4 D, ax, bX0, bX1; u32x16 A;
    auto first_operands = [&]() {
#pragma unroll
      for (int i = 0; i < 4; ++i) D[i] = lds_ld<u32>(aD + 128 * i + 12);
      const u32 dw = lds_ld<u32>(aR + 12);
      ax = lds_ld<u32x4>(U.w ? (dw >> 16) & 0xf0 : aR);
      bX0 = lds_ld<u32x4>(aB);
    };
    auto read_A = [&](u32 off) {
#pragma unroll
      for (int k = 0; k < 4; ++k) {
        const u32x4 a = lds_ld<u32x4>(K[4 + k] + off);
        A[4 * k] = a.x; A[4 * k + 1] = a.y; A[4 * k + 2] = a.z; A[4 * k + 3] = a.w;
      }
      asm volatile("s_waitcnt lgkmcnt(0)" : "+{v[224:239]}"(A)::"memory");
    };
    if (wave < 4) {
      gather(0, gbuf + 4 * 4096);
      gather(1, gbuf);
      first_operands();
      asm volatile("" : "=v"(acc), "=v"(wm), "=v"(hres));
      asm volatile("s_waitcnt vmcnt(4)" ::"v"(hwarm) : "memory");
      read_A(4 * 4096);
      sy_arrive(SY_PAIR + wave);
    } else {
      first_operands();
      mlp_prep();
      sy_wait(SY_PAIR + wave - 4, 1);
      gather(0, gbuf);
      asm volatile("s_waitcnt vmcnt(0)" ::"v"(hwarm) : "memory");
      read_A(0);
      gather(1, gbuf);
    }
    f32x16 X0;
#pragma unroll
    for (int i = 0; i < 16; ++i) X0[i] = 0.f;
    X0 = MFMA(ax, bX0, X0);
#pragma unroll
    for (int k = 0; k < 2; ++k) X0 = MFMA(sub4(A, k), (u32x4{ID[4 * k], ID[4 * k + 1], ID[4 * k + 2], ID[4 * k + 3]}), X0);

    int tile = t0;
    const int tlast = t1 - 1;
    while (tile < t1) {
      const int tend = min(t1, (int)__builtin_amdgcn_readlane(U.x, w + 1));
      int wn = w;
      if (tend < t1) { do { ++wn; } while ((int)__builtin_amdgcn_readlane(U.x, wn + 1) <= tend); }
      bX1 = lds_ld<u32x4>(U.z + U.w * w + 512);
      const u32 bnext = U.z + U.w * wn;
      asm volatile(TILE_LOOP_ASM
                   : "+{v[192:207]}"(X0), "+{v[224:239]}"(A),
                     "+{v[240:243]}"(ax), "+{v[244:247]}"(bX0), "+{v[252:255]}"(D), "+{v[96:103]}"(G),
                     "+{v[104:119]}"(PS), "+{v[48:55]}"(Q),
                     "+{v[40:43]}"(U), "+{v[64:79]}"(acc), "+{v[24:39]}"(wm), "+{v[56:63]}"(hres), "+{v[136:143]}"(EP), [tile] "+s"(tile)
                   : "{v[128:135]}"(ID), "{v[248:251]}"(bX1),
                     "{v[88:95]}"(K), "{v[80:82]}"(F), "{v86}"(bnext), [slab] "s"((order + w) * SLAB_BYTES), [tend] "s"(tend), [tlast] "s"(tlast), [tprio] "s"(tprio),
                     [rs] "s"(hrs), [gbuf] "s"(gbuf)
                   : TILE_LOOP_CLOBBERS);
      w = wn;
    }
  }
  asm volatile("s_waitcnt vmcnt(0)" ::: "memory");
  __syncthreads();
  if (wave < NWIN) {
    const u32 sa[3] = {EP[6], EP[7], U.y};
    u32 smask = __builtin_amdgcn_readlane(U.x, 8 + wave);
    smask &= smask - 1; smask &= smask - 1; smask &= smask - 1;
    f32x4 u = lds_ld<f32x4>(sa[0]) + lds_ld<f32x4>(sa[1]) + lds_ld<f32x4>(sa[2]);
    f32x4 v = lds_ld<f32x4>(sa[0] + 16) + lds_ld<f32x4>(sa[1] + 16) + lds_ld<f32x4>(sa[2] + 16);
    for (; __builtin_expect(smask != 0, 0); smask &= smask - 1) {
      const u32 a = L_SLAB + (wave + __builtin_ctz(smask)) * SLAB_BYTES + fresh_lane() * 32;
      u += lds_ld<f32x4>(a); v += lds_ld<f32x4>(a + 16);
    }
    u *= NEG_LN2; v *= NEG_LN2;
    lds_st<u32x4>(EP[4], u32x4{cvt_pk(u.x, u.y), cvt_pk(u.z, u.w), cvt_pk(v.x, v.y), cvt_pk(v.z, v.w)});
  }
  __syncthreads();
  if (wave >= 4) {
    u32x4 mf[4];
#pragma unroll
    for (int k = 0; k < 4; ++k) mf[k] = lds_ld<u32x4>(EP[k]);
    SCHED_FENCE();
#pragma unroll
    for (int k = 0; k < 4; ++k) acc = MFMA(sub4(wm, k), mf[k], acc);
    const __amdgpu_buffer_rsrc_t ro = __builtin_amdgcn_make_buffer_rsrc((void*)(c.out_h + c.d0 * 64), 0, c.nn * 128, 0x00020000);
#pragma unroll
    for (int g = 0; g < 4; ++g) {
      const u32 lo = cvt_pk(bf_lo(hres[2 * g]) + silu_f(acc[4 * g]), bf_hi(hres[2 * g]) + silu_f(acc[4 * g + 1]));
      const u32 hi = cvt_pk(bf_lo(hres[2 * g + 1]) + silu_f(acc[4 * g + 2]), bf_hi(hres[2 * g + 1]) + silu_f(acc[4 * g + 3]));
      __builtin_amdgcn_raw_buffer_store_b64(u32x2{lo, hi}, ro, EP[5] + 16 * g, 0, 0);
    }
  } else if (wave < 2) {
    const int x3 = wave * 64 + fresh_lane(), nl = min(x3 / 3, 39);
    u32 pm = __builtin_amdgcn_ds_bpermute(32 + 4 * (nl >> 3), (int)U.x);
    float u = 0.f;
    for (; pm; pm &= pm - 1) {
      const u32 a = L_SLAB + ((nl >> 3) + __builtin_ctz(pm)) * SLAB_BYTES + 2048 + ((nl & 7) * 4 + x3 - 3 * nl + 1) * 4;
      u += lds_ld<float>(a) + lds_ld<float>(a + 128);
    }
    if (x3 < 3 * c.nn) c.out_pos[c.d0 * 3 + x3] = (u16)cvt_pk(lds_ld<float>(L_PD + 4 * (x3 + nl)) + round_bf(u), 0.f);
  }
  }
}

extern "C" void kernel_launch(void* const* d_in, const int* in_sizes, int n_in, void* d_out, int out_size,
                              void* d_ws, size_t ws_size, hipStream_t stream) {
  const u16* h = (const u16*)d_in[0];
  const u16* pos = (const u16*)d_in[1];
  const u32x2* eattr = (const u32x2*)d_in[2];
  const int* src = (const int*)d_in[3];
  const int* dst = (const int*)d_in[4];
  const u16* w_e = (const u16*)d_in[5];
  const u16* w_x = (const u16*)d_in[6];
  const u16* w_h = (const u16*)d_in[7];
  const int N = in_sizes[0] / 64;
  const int E = in_sizes[3];
  if (N > NMAX || N < 3) return;
  const int nwg = N >= 32 * NWG_MAX ? NWG_MAX : (N + 39) / 40;
  const int base = N / nwg, rem = N % nwg;
  const int m1 = ((1 << 20) + base) / (base + 1), m2 = ((1 << 20) + base - 1) / base;
  const int chunk = ((E + nwg - 1) / nwg + 3) & ~3;
  if (chunk > CHUNK_MAX || ws_size < WS_HAND) return;
  u16* out_h = (u16*)d_out;
  u16* out_pos = out_h + (size_t)N * 64;
  (void)hipFuncSetAttribute((const void*)egnn_kernel, hipFuncAttributeMaxDynamicSharedMemorySize, L_TOTAL);
  egnn_kernel<<<nwg, NT, L_TOTAL, stream>>>(h, pos, eattr, src, dst, w_e, w_x, w_h, out_h, out_pos, (char*)d_ws, N, E, chunk, nwg, base, rem, m1, m2);
}
```
